```python
import torch

def _softmax(x, axis=-1):
    return torch.softmax(x, dim=axis)
def _gelu(x):
    return torch.nn.functional.gelu(x, approximate="tanh")
def _silu(x):
    return torch.nn.functional.silu(x)
def _sigmoid(x):
    return torch.sigmoid(x)
def _layernorm(x, axis=-1, eps=1e-5):
    m = x.mean(dim=axis, keepdim=True)
    v = x.var(dim=axis, keepdim=True, unbiased=False)
    return (x - m) / torch.sqrt(v + eps)
def _scatter_add(tgt, idx, src, axis=0):
    return tgt.clone().index_add_(axis, idx, src)
def _segment_sum(src, seg, n):
    out = torch.zeros((n,) + src.shape[1:], dtype=src.dtype)
    return out.index_add_(0, seg, src)
def _pad(x, pw):
    flat = tuple(v for pair in reversed(pw) for v in pair)
    return torch.nn.functional.pad(x, flat)

kernel_name = 'standalone__self_sup_conv_stem_wavlm__speech'

def setup_inputs():

    _w = (lambda a: (lambda t: t.to(torch.bfloat16) if t.is_floating_point() else t)(torch.as_tensor(a)))
    B, T, D, K = 2, 16000, 256, 31
    scale = (K * D) ** -0.5
    return {
        "x": _w((lambda *s: torch.randn(*s, dtype=torch.bfloat16) * 0.05)(B, T, D)),
        "w": _w((lambda *s: torch.randn(*s, dtype=torch.bfloat16) * 0.05)(K, D, D) * scale),
        "gn_gamma": _w((lambda *s: torch.randn(*s, dtype=torch.bfloat16) * 0.05)(D) * 0.1 + 1.0),
        "gn_beta": _w((lambda *s: torch.randn(*s, dtype=torch.bfloat16) * 0.05)(D) * 0.1),
    }

def reference(x, w, gn_gamma, gn_beta):
    B, T, D = x.shape
    K = w.shape[0]
    s = 2
    L = (T - K) // s + 1
    h = torch.einsum('blc,cd->bld', x[:, 0:s * L:s, :], w[0])
    for i in range(1, K):
        h = h + torch.einsum('blc,cd->bld', x[:, i:i + s * L:s, :], w[i])
    mu = torch.einsum('bld->bd', h)[:, None, :] / L
    var = torch.einsum('bld->bd', (h - mu) ** 2)[:, None, :] / L
    h = (h - mu) / torch.sqrt(var + 1e-5)
    h = h * gn_gamma[None, None, :] + gn_beta[None, None, :]
    return _gelu(h)
```

```cpp
#include <hip/hip_runtime.h>
#include <hip/hip_bf16.h>
typedef __attribute__((ext_vector_type(8))) __bf16 bf16x8;
typedef __attribute__((ext_vector_type(4))) float f32x4;
typedef __attribute__((ext_vector_type(4))) unsigned u32x4;
constexpr int Bn=2, T=16000, D=256, KT=31, L=7985;

__global__ __launch_bounds__(256) void conv_k(const unsigned short* __restrict__ x, const unsigned short* __restrict__ w, float* __restrict__ h) {
  int wv = threadIdx.x>>6, lane = threadIdx.x&63, g = lane>>4, r = lane&15;
  int rt = blockIdx.x;
  int b = blockIdx.y;
  int cb = blockIdx.z;
  int n0 = cb*128 + wv*32;
  int l0 = rt*64;
  f32x4 acc[4][2] = {};
  const unsigned short* xb = x + (size_t)b*T*D;
  for (int i=0;i<KT;i++) {
    for (int c0=0;c0<D;c0+=32) {
      bf16x8 wf[2];
      for (int f=0;f<2;f++) {
        unsigned short tmp[8];
        for (int e=0;e<8;e++) tmp[e] = w[((size_t)i*D + c0+8*g+e)*D + n0 + 2*r + f];
        wf[f] = *(bf16x8*)tmp;
      }
      for (int m=0;m<4;m++) {
        int l = l0 + m*16 + r; if (l>L-1) l = L-1;
        bf16x8 xf = *(const bf16x8*)(xb + (size_t)(2*l+i)*D + c0 + 8*g);
        for (int f=0;f<2;f++) acc[m][f] = __builtin_amdgcn_mfma_f32_16x16x32_bf16(wf[f], xf, acc[m][f], 0,0,0);
      }
    }
  }
  for (int m=0;m<4;m++) {
    int l = l0 + m*16 + r;
    if (l<L) for (int f=0;f<2;f++) for (int q=0;q<4;q++) h[((size_t)b*L + l)*D + n0 + 8*g + 2*q + f] = acc[m][f][q];
  }
}
__global__ __launch_bounds__(256) void stats_k(const float* __restrict__ h, float* __restrict__ st) {
  int b = blockIdx.y, d = blockIdx.x;
  __shared__ float red[256];
  float s=0; for (int l=threadIdx.x;l<L;l+=256) s += h[((size_t)b*L+l)*D+d];
  red[threadIdx.x]=s; __syncthreads();
  for (int o=128;o>0;o>>=1){ if(threadIdx.x<o) red[threadIdx.x]+=red[threadIdx.x+o]; __syncthreads(); }
  float mu = red[0]/L; __syncthreads();
  s=0; for (int l=threadIdx.x;l<L;l+=256) { float v=h[((size_t)b*L+l)*D+d]-mu; s+=v*v; }
  red[threadIdx.x]=s; __syncthreads();
  for (int o=128;o>0;o>>=1){ if(threadIdx.x<o) red[threadIdx.x]+=red[threadIdx.x+o]; __syncthreads(); }
  if (threadIdx.x==0){ st[(b*D+d)*2]=mu; st[(b*D+d)*2+1]=rsqrtf(red[0]/L+1e-5f); }
}
__global__ __launch_bounds__(256) void norm_k(const float* __restrict__ h, const float* __restrict__ st, const __hip_bfloat16* gam, const __hip_bfloat16* bet, __hip_bfloat16* out) {
  size_t idx = (size_t)blockIdx.x*256+threadIdx.x;
  int d = threadIdx.x; int b = blockIdx.x / L;
  float z = (h[idx]-st[(b*D+d)*2])*st[(b*D+d)*2+1]*__bfloat162float(gam[d])+__bfloat162float(bet[d]);
  float u = 0.7978845608028654f*(z+0.044715f*z*z*z);
  float o = z/(1.f+__expf(-2.f*u));
  out[idx] = __float2bfloat16(o);
}
extern "C" void kernel_launch(void* const* d_in, const int* in_sizes, int n_in, void* d_out, int out_size, void* d_ws, size_t ws_size, hipStream_t stream) {
  float* h = (float*)d_ws; float* st = h + (size_t)Bn*L*D;
  conv_k<<<dim3(125,2,2),256,0,stream>>>((const unsigned short*)d_in[0], (const unsigned short*)d_in[1], h);
  stats_k<<<dim3(256,2),256,0,stream>>>(h, st);
  norm_k<<<Bn*L,256,0,stream>>>(h, st, (const __hip_bfloat16*)d_in[2], (const __hip_bfloat16*)d_in[3], (__hip_bfloat16*)d_out);
}
```

```cpp
#include <hip/hip_runtime.h>
constexpr int T_ = 16000, L_ = 7985;
constexpr int WP_BYTES = 8 * 16 * 56 * 1024;
constexpr int GR_OFF = WP_BYTES;
constexpr int FL_OFF = GR_OFF + 16 * 8192;
constexpr int EP_OFF = FL_OFF + 65536;

__global__ __launch_bounds__(512, 2) void conv_gn_gelu_k(const unsigned char* __restrict__ x, const unsigned char* __restrict__ w,
                                                         unsigned char* __restrict__ ws, const unsigned short* __restrict__ gam,
                                                         const unsigned short* __restrict__ bet, unsigned char* __restrict__ out) {
  const int bid = blockIdx.x;
  const int xcd = bid & 7, idx = bid >> 3;
  const int cb = idx & 7, rtg = xcd * 4 + (idx >> 3);
  const int b = rtg >> 4, rt = rtg & 15;
  const int wv = __builtin_amdgcn_readfirstlane(threadIdx.x >> 6);
  unsigned tag;
  const unsigned long long xb = (unsigned long long)(x + (size_t)b * T_ * 512);
  const unsigned long long wb = (unsigned long long)(ws + (size_t)cb * 16 * 56 * 1024);
  const unsigned long long gb = (unsigned long long)w;
  const unsigned long long fb = (unsigned long long)(ws + FL_OFF + cb * 8192);
  const unsigned long long rb = (unsigned long long)(ws + GR_OFF + (size_t)(b * 8 + cb) * 8192);
  const unsigned long long ob = (unsigned long long)(out + (size_t)b * L_ * 512);
  asm volatile(
      "s_cmp_lg_u32 %[wv], 7\n"
      "s_cbranch_scc1 LCLR%=\n"
      "v_mov_b32 v2, 163776\n"
      "v_mov_b32 v0, 0\n"
      "v_mov_b32 v1, 0\n"
      "ds_write_b64 v2, v[0:1]\n"
      "s_waitcnt lgkmcnt(0)\n"
      "LCLR%=:\n"
      "s_getpc_b64 s[84:85]\n"
      "s_lshl_b32 s87, %[cj], 6\n"
      "s_load_dword s86, s[84:85], s87\n"
      "v_mbcnt_lo_u32_b32 v7, -1, 0\n"
      "v_mbcnt_hi_u32_b32 v7, -1, v7\n"
      "s_mul_i32 s48, %[rt], 512000\n"
      "s_mov_b32 s40, %[xlo]\n"
      "s_mov_b32 s41, %[xhi]\n"
      "s_mov_b32 s42, 8191488\n"
      "s_mov_b32 s43, 0x20000\n"
      "s_cmp_lt_u32 %[wv], 4\n"
      "s_cbranch_scc1 LCONS%=\n"
      "s_sub_u32 s62, %[wv], 4\n"
      "s_lshl_b32 s49, %[cb], 16\n"
      "s_add_u32 s49, s49, s48\n"
      "s_lshl_b32 s50, s62, 14\n"
      "s_add_u32 s49, s49, s50\n"
      "v_and_b32 v2, 1, v7\n"
      "v_lshlrev_b32 v2, 7, v2\n"
      "v_lshrrev_b32 v3, 2, v7\n"
      "v_lshl_add_u32 v2, v3, 10, v2\n"
      "v_add_u32 v2, s49, v2\n"
      "s_sleep 0\n"
      "buffer_load_dword v244, v2, s[40:43], 0 offen nt\n"
      "s_barrier\n"
      "s_branch LPROD%=\n"
      "LCONS%=:\n"
      "s_load_dword %[tag], %[ep], 0x0\n"
      "v_lshlrev_b32 v4, 4, v7\n"
      "s_mov_b32 s64, %[glo]\n"
      "s_mov_b32 s65, %[ghi]\n"
      "s_mov_b32 s66, 4063232\n"
      "s_mov_b32 s67, 0x20000\n"
      "s_xor_b32 s73, %[wv], 1\n"
      "s_cmp_lt_u32 %[wv], 2\n"
      "s_cselect_b32 s73, s73, %[wv]\n"
      "s_lshr_b32 s49, %[ci], 3\n"
      "s_lshl_b32 s50, s73, 2\n"
      "s_add_u32 s49, s49, s50\n"
      "s_bfe_u32 s50, %[ci], 0x20001\n"
      "s_and_b32 s51, %[ci], 1\n"
      "s_lshl_b32 s70, s49, 14\n"
      "s_lshl_b32 s71, s50, 20\n"
      "s_add_u32 s71, s71, s70\n"
      "s_lshl_b32 s70, %[cb], 6\n"
      "s_add_u32 s71, s71, s70\n"
      "v_lshrrev_b32 v0, 4, v7\n"
      "v_and_b32 v1, 15, v7\n"
      "v_lshlrev_b32 v0, 12, v0\n"
      "v_lshl_add_u32 v0, v1, 2, v0\n"
      "v_add_u32 v0, s71, v0\n"
      "s_cmp_eq_u32 %[wv], 1\n"
      "s_cbranch_scc1 LNS%=\n"
      "s_barrier\n"
      "s_sleep 56\n"
      "LNS%=:\n"
      "v_add_u32 v1, 262144, v0\n"
      "v_add_u32 v2, 524288, v0\n"
      "v_add_u32 v3, 786432, v0\n"
      "buffer_load_dword v124, v0, s[64:67], 0 offen offset:0 nt\n"
      "buffer_load_dword v125, v0, s[64:67], 0 offen offset:512 nt\n"
      "buffer_load_dword v126, v0, s[64:67], 0 offen offset:1024 nt\n"
      "buffer_load_dword v127, v0, s[64:67], 0 offen offset:1536 nt\n"
      "buffer_load_dword v128, v0, s[64:67], 0 offen offset:2048 nt\n"
      "buffer_load_dword v129, v0, s[64:67], 0 offen offset:2560 nt\n"
      "buffer_load_dword v130, v0, s[64:67], 0 offen offset:3072 nt\n"
      "buffer_load_dword v131, v0, s[64:67], 0 offen offset:3584 nt\n"
      "buffer_load_dword v132, v1, s[64:67], 0 offen offset:0 nt\n"
      "buffer_load_dword v133, v1, s[64:67], 0 offen offset:512 nt\n"
      "buffer_load_dword v134, v1, s[64:67], 0 offen offset:1024 nt\n"
      "buffer_load_dword v135, v1, s[64:67], 0 offen offset:1536 nt\n"
      "buffer_load_dword v136, v1, s[64:67], 0 offen offset:2048 nt\n"
      "buffer_load_dword v137, v1, s[64:67], 0 offen offset:2560 nt\n"
      "buffer_load_dword v138, v1, s[64:67], 0 offen offset:3072 nt\n"
      "buffer_load_dword v139, v1, s[64:67], 0 offen offset:3584 nt\n"
      "buffer_load_dword v140, v2, s[64:67], 0 offen offset:0 nt\n"
      "buffer_load_dword v141, v2, s[64:67], 0 offen offset:512 nt\n"
      "buffer_load_dword v142, v2, s[64:67], 0 offen offset:1024 nt\n"
      "buffer_load_dword v143, v2, s[64:67], 0 offen offset:1536 nt\n"
      "buffer_load_dword v144, v2, s[64:67], 0 offen offset:2048 nt\n"
      "buffer_load_dword v145, v2, s[64:67], 0 offen offset:2560 nt\n"
      "buffer_load_dword v146, v2, s[64:67], 0 offen offset:3072 nt\n"
      "buffer_load_dword v147, v2, s[64:67], 0 offen offset:3584 nt\n"
      "buffer_load_dword v148, v3, s[64:67], 0 offen offset:0 nt\n"
      "buffer_load_dword v149, v3, s[64:67], 0 offen offset:512 nt\n"
      "buffer_load_dword v150, v3, s[64:67], 0 offen offset:1024 nt\n"
      "buffer_load_dword v151, v3, s[64:67], 0 offen offset:1536 nt\n"
      "buffer_load_dword v152, v3, s[64:67], 0 offen offset:2048 nt\n"
      "buffer_load_dword v153, v3, s[64:67], 0 offen offset:2560 nt\n"
      "buffer_load_dword v154, v3, s[64:67], 0 offen offset:3072 nt\n"
      "buffer_load_dword v155, v3, s[64:67], 0 offen offset:3584 nt\n"
      "s_cmp_lg_u32 %[wv], 1\n"
      "s_cbranch_scc1 LNOH%=\n"
      "s_add_u32 s56, s48, 524288\n"
      "v_add_u32 v5, s56, v4\n"
      "buffer_load_dwordx4 v[212:215], v5, s[40:43], 0 offen offset:0 nt\n"
      "buffer_load_dwordx4 v[216:219], v5, s[40:43], 0 offen offset:1024 nt\n"
      "buffer_load_dwordx4 v[220:223], v5, s[40:43], 0 offen offset:2048 nt\n"
      "s_barrier\n"
      "LNOH%=:\n"
      "s_mov_b32 s44, %[wlo]\n"
      "s_mov_b32 s45, %[whi]\n"
      "s_mov_b32 s46, 917504\n"
      "s_mov_b32 s47, 0x20000\n"
      "s_mov_b32 s76, %[flo]\n"
      "s_mov_b32 s77, %[fhi]\n"
      "s_movk_i32 s78, 8192\n"
      "s_mov_b32 s79, 0x20000\n"
      "s_mul_i32 s72, s51, 0x02020000\n"
      "s_add_u32 s84, s72, 0x01000c0c\n"
      "s_mul_i32 s70, s49, 57344\n"
      "s_and_b32 s68, %[ci], 7\n"
      "s_lshl_b32 s68, s68, 10\n"
      "s_add_u32 s68, s68, s70\n"
      "s_lshl_b32 s69, s51, 2\n"
      "s_add_u32 s69, s69, s50\n"
      "s_lshl_b32 s69, s69, 10\n"
      "s_add_u32 s69, s69, s70\n"
      "s_mov_b32 s88, 0x3f610000\n"
      "s_mov_b32 s89, 0x3f700000\n"
      "s_mov_b32 s90, 0x40a20000\n"
      "s_mov_b32 s91, 0x40100000\n"
      "s_mov_b32 s92, 0xc1735c29\n"
      "s_mov_b32 s93, 0x4200b5dd\n"
      "s_mov_b32 s83, 0xffff0000\n"
      "s_lshl_b32 s49, %[wv], 13\n"
      "s_add_u32 s50, s49, 4096\n"
      "s_lshl_b32 s51, %[wv], 12\n"
      "s_add_u32 s51, s51, 32768\n"
      "s_and_b32 s52, %[wv], 1\n"
      "s_lshl_b32 s52, s52, 12\n"
      "s_add_u32 s52, s52, 49152\n"
      "s_mov_b32 s54, 4294909952\n"
      "s_mov_b32 s55, 15\n"
      "s_setprio 1\n"
      "v_lshrrev_b32 v12, 4, v7\n"
      "v_and_b32 v13, 15, v7\n"
      "v_cmp_eq_u32 vcc, 15, v13\n"
      "v_lshrrev_b32 v14, 2, v13\n"
      "v_and_b32 v14, 2, v14\n"
      "v_xor_b32 v14, v14, v12\n"
      "v_lshlrev_b32 v14, 4, v14\n"
      "v_lshl_add_u32 v14, v13, 6, v14\n"
      "v_add_u32 v13, 1, v13\n"
      "v_lshrrev_b32 v15, 2, v13\n"
      "v_and_b32 v15, 2, v15\n"
      "v_xor_b32 v15, v15, v12\n"
      "v_lshlrev_b32 v15, 4, v15\n"
      "v_lshl_add_u32 v15, v13, 6, v15\n"
      "v_lshlrev_b32 v16, 4, v12\n"
      "v_add_u32 v16, 154624, v16\n"
      "s_lshl_b32 s56, %[wv], 13\n"
      "s_add_u32 s56, s56, 57344\n"
      "v_add_u32 v0, s56, v14\n"
      "v_add_u32 v1, s56, v15\n"
      "s_lshl_b32 s56, %[wv], 6\n"
      "v_add_u32 v17, s56, v16\n"
      "v_cndmask_b32 v1, v1, v17, vcc\n"
      "s_lshr_b32 s57, %[wv], 1\n"
      "s_add_u32 s57, s57, 4\n"
      "s_lshl_b32 s56, s57, 13\n"
      "s_add_u32 s56, s56, 57344\n"
      "v_add_u32 v2, s56, v14\n"
      "v_add_u32 v3, s56, v15\n"
      "s_lshl_b32 s56, s57, 6\n"
      "v_add_u32 v17, s56, v16\n"
      "v_cndmask_b32 v3, v3, v17, vcc\n"
      "s_mov_b32 s57, 6\n"
      "s_lshl_b32 s56, s57, 13\n"
      "s_add_u32 s56, s56, 57344\n"
      "v_add_u32 v5, s56, v14\n"
      "v_add_u32 v6, s56, v15\n"
      "s_lshl_b32 s56, s57, 6\n"
      "v_add_u32 v17, s56, v16\n"
      "v_cndmask_b32 v6, v6, v17, vcc\n"
      "v_mov_b32 v8, 4294909952\n"
      "v_mov_b32 v17, 448\n"
      "v_cndmask_b32 v8, v8, v17, vcc\n"
      "v_mov_b32 v9, 0\n"
      "v_mov_b32 v17, 896\n"
      "v_cndmask_b32 v9, v9, v17, vcc\n"
      "v_mov_b32 v10, v8\n"
      "v_mov_b32 v11, v9\n"
      "s_cmp_lt_u32 %[wv], 2\n"
      "s_cbranch_scc0 LH1%=\n"
      "v_add_u32 v6, 4096, v5\n"
      "v_mov_b32 v10, 4294909952\n"
      "v_mov_b32 v11, 0\n"
      "s_branch LH0%=\n"
      "LH1%=:\n"
      "v_add_u32 v5, 4096, v5\n"
      "LH0%=:\n"
      "s_waitcnt lgkmcnt(0)\n"
      "s_add_u32 %[tag], %[tag], 1\n"
      "v_mov_b32 v204, %[tag]\n"
      "v_not_b32 v205, v204\n"
      "v_mov_b32 v207, 163776\n"
      "s_mov_b32 s71, 0x200000\n"
      "s_lshl_b32 s81, %[cb], 16\n"
      "s_add_u32 s81, s81, s48\n"
      "s_lshl_b32 s82, %[wv], 14\n"
      "s_add_u32 s81, s81, s82\n"
      "s_add_u32 s81, s81, 12604\n"
      "s_min_u32 s81, s81, 8179200\n"
      "s_movk_i32 s82, 2048\n"
      "s_lshl_b32 s74, s73, 8\n"
      "s_lshl_b32 s56, %[ci], 3\n"
      "s_add_u32 s74, s74, s56\n"
      "s_lshl_b32 s75, s73, 3\n"
      "s_lshr_b32 s75, 0x80d0f11, s75\n"
      "s_and_b32 s75, s75, 0xff\n"
      "s_sub_u32 s73, s75, 1\n"
      "s_mov_b32 s80, 0\n"
      "s_movk_i32 s72, 256\n"
      "v_mov_b64 v[12:13], 0\n"
      "v_mov_b64 v[14:15], 0\n"
      "v_mov_b64 v[16:17], 0\n"
      "v_mov_b64 v[18:19], 0\n"
      "v_mov_b64 v[20:21], 0\n"
      "v_mov_b64 v[22:23], 0\n"
      "v_mov_b64 v[24:25], 0\n"
      "v_mov_b64 v[26:27], 0\n"
      "v_mov_b64 v[28:29], 0\n"
      "v_mov_b64 v[30:31], 0\n"
      "v_mov_b64 v[32:33], 0\n"
      "v_mov_b64 v[34:35], 0\n"
      "v_mov_b64 v[36:37], 0\n"
      "v_mov_b64 v[38:39], 0\n"
      "v_mov_b64 v[40:41], 0\n"
      "v_mov_b64 v[42:43], 0\n"
      "v_mov_b64 v[44:45], 0\n"
      "v_mov_b64 v[46:47], 0\n"
      "v_mov_b64 v[48:49], 0\n"
      "v_mov_b64 v[50:51], 0\n"
      "v_mov_b64 v[52:53], 0\n"
      "v_mov_b64 v[54:55], 0\n"
      "v_mov_b64 v[56:57], 0\n"
      "v_mov_b64 v[58:59], 0\n"
      "v_mov_b64 v[60:61], 0\n"
      "v_mov_b64 v[62:63], 0\n"
      "v_mov_b64 v[64:65], 0\n"
      "v_mov_b64 v[66:67], 0\n"
      "v_mov_b64 v[68:69], 0\n"
      "v_mov_b64 v[70:71], 0\n"
      "v_mov_b64 v[72:73], 0\n"
      "v_mov_b64 v[74:75], 0\n"
      "v_mov_b64 v[76:77], 0\n"
      "v_mov_b64 v[78:79], 0\n"
      "v_mov_b64 v[80:81], 0\n"
      "v_mov_b64 v[82:83], 0\n"
      "v_mov_b64 v[84:85], 0\n"
      "v_mov_b64 v[86:87], 0\n"
      "v_mov_b64 v[88:89], 0\n"
      "v_mov_b64 v[90:91], 0\n"
      "v_mov_b64 v[92:93], 0\n"
      "v_mov_b64 v[94:95], 0\n"
      "v_mov_b64 v[96:97], 0\n"
      "v_mov_b64 v[98:99], 0\n"
      "v_mov_b64 v[100:101], 0\n"
      "v_mov_b64 v[102:103], 0\n"
      "v_mov_b64 v[104:105], 0\n"
      "v_mov_b64 v[106:107], 0\n"
      "v_mov_b64 v[108:109], 0\n"
      "v_mov_b64 v[110:111], 0\n"
      "v_mov_b64 v[112:113], 0\n"
      "v_mov_b64 v[114:115], 0\n"
      "v_mov_b64 v[116:117], 0\n"
      "v_mov_b64 v[118:119], 0\n"
      "v_mov_b64 v[120:121], 0\n"
      "v_mov_b64 v[122:123], 0\n"
      "s_waitcnt vmcnt(0)\n"
      "s_cmp_eq_u32 %[wv], 1\n"
      "s_cbranch_scc0 LNURG%=\n"
      "v_perm_b32 v148, v148, v148, s84\n"
      "v_perm_b32 v149, v149, v149, s84\n"
      "v_perm_b32 v150, v150, v150, s84\n"
      "v_perm_b32 v151, v151, v151, s84\n"
      "v_perm_b32 v152, v152, v152, s84\n"
      "v_perm_b32 v153, v153, v153, s84\n"
      "v_perm_b32 v154, v154, v154, s84\n"
      "v_perm_b32 v155, v155, v155, s84\n"
      "v_cvt_pk_bf16_f32 v244, v148, v149\n"
      "v_cvt_pk_bf16_f32 v245, v150, v151\n"
      "v_cvt_pk_bf16_f32 v246, v152, v153\n"
      "v_cvt_pk_bf16_f32 v247, v154, v155\n"
      "s_add_u32 s70, s69, 32768\n"
      "buffer_store_dwordx4 v[244:247], v4, s[44:47], s70 offen sc1\n"
      "v_perm_b32 v132, v132, v132, s84\n"
      "v_perm_b32 v133, v133, v133, s84\n"
      "v_perm_b32 v134, v134, v134, s84\n"
      "v_perm_b32 v135, v135, v135, s84\n"
      "v_perm_b32 v136, v136, v136, s84\n"
      "v_perm_b32 v137, v137, v137, s84\n"
      "v_perm_b32 v138, v138, v138, s84\n"
      "v_perm_b32 v139, v139, v139, s84\n"
      "v_pk_fma_f32 v[156:157], v[132:133], s[88:89], v[148:149] op_sel:[0,0,0] op_sel_hi:[1,0,1]\n"
      "v_pk_fma_f32 v[158:159], v[134:135], s[88:89], v[150:151] op_sel:[0,0,0] op_sel_hi:[1,0,1]\n"
      "v_pk_fma_f32 v[160:161], v[136:137], s[88:89], v[152:153] op_sel:[0,0,0] op_sel_hi:[1,0,1]\n"
      "v_pk_fma_f32 v[162:163], v[138:139], s[88:89], v[154:155] op_sel:[0,0,0] op_sel_hi:[1,0,1]\n"
      "v_pk_fma_f32 v[172:173], v[132:133], s[90:91], v[148:149] op_sel:[0,0,0] op_sel_hi:[1,0,1]\n"
      "v_pk_fma_f32 v[174:175], v[134:135], s[90:91], v[150:151] op_sel:[0,0,0] op_sel_hi:[1,0,1]\n"
      "v_pk_fma_f32 v[176:177], v[136:137], s[90:91], v[152:153] op_sel:[0,0,0] op_sel_hi:[1,0,1]\n"
      "v_pk_fma_f32 v[178:179], v[138:139], s[90:91], v[154:155] op_sel:[0,0,0] op_sel_hi:[1,0,1]\n"
      "v_perm_b32 v124, v124, v124, s84\n"
      "v_perm_b32 v125, v125, v125, s84\n"
      "v_perm_b32 v126, v126, v126, s84\n"
      "v_perm_b32 v127, v127, v127, s84\n"
      "v_perm_b32 v128, v128, v128, s84\n"
      "v_perm_b32 v129, v129, v129, s84\n"
      "v_perm_b32 v130, v130, v130, s84\n"
      "v_perm_b32 v131, v131, v131, s84\n"
      "v_cvt_pk_bf16_f32 v248, v124, v125\n"
      "v_cvt_pk_bf16_f32 v249, v126, v127\n"
      "v_cvt_pk_bf16_f32 v250, v128, v129\n"
      "v_cvt_pk_bf16_f32 v251, v130, v131\n"
      "s_add_u32 s70, s69, 40960\n"
      "buffer_store_dwordx4 v[248:251], v4, s[44:47], s70 offen sc1\n"
      "v_perm_b32 v140, v140, v140, s84\n"
      "v_perm_b32 v141, v141, v141, s84\n"
      "v_perm_b32 v142, v142, v142, s84\n"
      "v_perm_b32 v143, v143, v143, s84\n"
      "v_perm_b32 v144, v144, v144, s84\n"
      "v_perm_b32 v145, v145, v145, s84\n"
      "v_perm_b32 v146, v146, v146, s84\n"
      "v_perm_b32 v147, v147, v147, s84\n"
      "v_pk_fma_f32 v[164:165], v[124:125], s[88:89], v[140:141] op_sel:[0,0,0] op_sel_hi:[1,0,1]\n"
      "v_pk_fma_f32 v[166:167], v[126:127], s[88:89], v[142:143] op_sel:[0,0,0] op_sel_hi:[1,0,1]\n"
      "v_pk_fma_f32 v[168:169], v[128:129], s[88:89], v[144:145] op_sel:[0,0,0] op_sel_hi:[1,0,1]\n"
      "v_pk_fma_f32 v[170:171], v[130:131], s[88:89], v[146:147] op_sel:[0,0,0] op_sel_hi:[1,0,1]\n"
      "v_pk_fma_f32 v[188:189], v[164:165], s[88:89], v[156:157] op_sel:[0,1,0] op_sel_hi:[1,1,1]\n"
      "v_pk_fma_f32 v[190:191], v[166:167], s[88:89], v[158:159] op_sel:[0,1,0] op_sel_hi:[1,1,1]\n"
      "v_pk_fma_f32 v[192:193], v[168:169], s[88:89], v[160:161] op_sel:[0,1,0] op_sel_hi:[1,1,1]\n"
      "v_pk_fma_f32 v[194:195], v[170:171], s[88:89], v[162:163] op_sel:[0,1,0] op_sel_hi:[1,1,1]\n"
      "v_cvt_pk_bf16_f32 v228, v188, v189\n"
      "v_cvt_pk_bf16_f32 v229, v190, v191\n"
      "v_cvt_pk_bf16_f32 v230, v192, v193\n"
      "v_cvt_pk_bf16_f32 v231, v194, v195\n"
      "s_add_u32 s70, s68, 0\n"
      "buffer_store_dwordx4 v[228:231], v4, s[44:47], s70 offen sc1\n"
      "v_pk_fma_f32 v[188:189], v[164:165], s[88:89], v[156:157] op_sel:[0,1,0] op_sel_hi:[1,1,1] neg_lo:[1,0,0] neg_hi:[1,0,0]\n"
      "v_pk_fma_f32 v[190:191], v[166:167], s[88:89], v[158:159] op_sel:[0,1,0] op_sel_hi:[1,1,1] neg_lo:[1,0,0] neg_hi:[1,0,0]\n"
      "v_pk_fma_f32 v[192:193], v[168:169], s[88:89], v[160:161] op_sel:[0,1,0] op_sel_hi:[1,1,1] neg_lo:[1,0,0] neg_hi:[1,0,0]\n"
      "v_pk_fma_f32 v[194:195], v[170:171], s[88:89], v[162:163] op_sel:[0,1,0] op_sel_hi:[1,1,1] neg_lo:[1,0,0] neg_hi:[1,0,0]\n"
      "v_cvt_pk_bf16_f32 v232, v188, v189\n"
      "v_cvt_pk_bf16_f32 v233, v190, v191\n"
      "v_cvt_pk_bf16_f32 v234, v192, v193\n"
      "v_cvt_pk_bf16_f32 v235, v194, v195\n"
      "s_add_u32 s70, s68, 8192\n"
      "buffer_store_dwordx4 v[232:235], v4, s[44:47], s70 offen sc1\n"
      "v_pk_fma_f32 v[180:181], v[124:125], s[90:91], v[140:141] op_sel:[0,0,0] op_sel_hi:[1,0,1]\n"
      "v_pk_fma_f32 v[182:183], v[126:127], s[90:91], v[142:143] op_sel:[0,0,0] op_sel_hi:[1,0,1]\n"
      "v_pk_fma_f32 v[184:185], v[128:129], s[90:91], v[144:145] op_sel:[0,0,0] op_sel_hi:[1,0,1]\n"
      "v_pk_fma_f32 v[186:187], v[130:131], s[90:91], v[146:147] op_sel:[0,0,0] op_sel_hi:[1,0,1]\n"
      "v_pk_fma_f32 v[188:189], v[180:181], s[90:91], v[172:173] op_sel:[0,1,0] op_sel_hi:[1,1,1] neg_lo:[1,0,0] neg_hi:[1,0,0]\n"
      "v_pk_fma_f32 v[190:191], v[182:183], s[90:91], v[174:175] op_sel:[0,1,0] op_sel_hi:[1,1,1] neg_lo:[1,0,0] neg_hi:[1,0,0]\n"
      "v_pk_fma_f32 v[192:193], v[184:185], s[90:91], v[176:177] op_sel:[0,1,0] op_sel_hi:[1,1,1] neg_lo:[1,0,0] neg_hi:[1,0,0]\n"
      "v_pk_fma_f32 v[194:195], v[186:187], s[90:91], v[178:179] op_sel:[0,1,0] op_sel_hi:[1,1,1] neg_lo:[1,0,0] neg_hi:[1,0,0]\n"
      "v_cvt_pk_bf16_f32 v240, v188, v189\n"
      "v_cvt_pk_bf16_f32 v241, v190, v191\n"
      "v_cvt_pk_bf16_f32 v242, v192, v193\n"
      "v_cvt_pk_bf16_f32 v243, v194, v195\n"
      "s_add_u32 s70, s68, 24576\n"
      "buffer_store_dwordx4 v[240:243], v4, s[44:47], s70 offen sc1\n"
      "v_pk_fma_f32 v[196:197], v[180:181], s[90:91], v[172:173] op_sel:[0,1,0] op_sel_hi:[1,1,1]\n"
      "v_pk_fma_f32 v[198:199], v[182:183], s[90:91], v[174:175] op_sel:[0,1,0] op_sel_hi:[1,1,1]\n"
      "v_pk_fma_f32 v[200:201], v[184:185], s[90:91], v[176:177] op_sel:[0,1,0] op_sel_hi:[1,1,1]\n"
      "v_pk_fma_f32 v[202:203], v[186:187], s[90:91], v[178:179] op_sel:[0,1,0] op_sel_hi:[1,1,1]\n"
      "v_cvt_pk_bf16_f32 v236, v196, v197\n"
      "v_cvt_pk_bf16_f32 v237, v198, v199\n"
      "v_cvt_pk_bf16_f32 v238, v200, v201\n"
      "v_cvt_pk_bf16_f32 v239, v202, v203\n"
      "s_add_u32 s70, s68, 16384\n"
      "buffer_store_dwordx4 v[236:239], v4, s[44:47], s70 offen sc1\n"
      "v_pk_fma_f32 v[196:197], v[164:165], s[92:93], v[196:197] op_sel:[0,0,0] op_sel_hi:[1,0,1]\n"
      "v_pk_fma_f32 v[198:199], v[166:167], s[92:93], v[198:199] op_sel:[0,0,0] op_sel_hi:[1,0,1]\n"
      "v_pk_fma_f32 v[200:201], v[168:169], s[92:93], v[200:201] op_sel:[0,0,0] op_sel_hi:[1,0,1]\n"
      "v_pk_fma_f32 v[202:203], v[170:171], s[92:93], v[202:203] op_sel:[0,0,0] op_sel_hi:[1,0,1]\n"
      "v_pk_fma_f32 v[196:197], v[148:149], s[92:93], v[196:197] op_sel:[0,1,0] op_sel_hi:[1,1,1]\n"
      "v_pk_fma_f32 v[198:199], v[150:151], s[92:93], v[198:199] op_sel:[0,1,0] op_sel_hi:[1,1,1]\n"
      "v_pk_fma_f32 v[200:201], v[152:153], s[92:93], v[200:201] op_sel:[0,1,0] op_sel_hi:[1,1,1]\n"
      "v_pk_fma_f32 v[202:203], v[154:155], s[92:93], v[202:203] op_sel:[0,1,0] op_sel_hi:[1,1,1]\n"
      "v_cvt_pk_bf16_f32 v252, v196, v197\n"
      "v_cvt_pk_bf16_f32 v253, v198, v199\n"
      "v_cvt_pk_bf16_f32 v254, v200, v201\n"
      "v_cvt_pk_bf16_f32 v255, v202, v203\n"
      "s_add_u32 s70, s69, 49152\n"
      "buffer_store_dwordx4 v[252:255], v4, s[44:47], s70 offen sc1\n"
      "v_lshrrev_b32 v210, 2, v7\n"
      "v_mul_u32_u24 v210, 448, v210\n"
      "v_and_b32 v211, 48, v4\n"
      "v_add_u32 v210, v210, v211\n"
      "v_add_u32 v210, 154624, v210\n"
      "v_mov_b64 v[224:225], 0\n"
      "v_mov_b64 v[226:227], 0\n"
      "v_and_b32 v206, 31, v7\n"
      "v_lshlrev_b32 v206, 7, v206\n"
      "v_add_u32 v206, 4096, v206\n"
      "s_lshl_b32 s74, %[ci], 7\n"
      "s_add_u32 s74, s74, 4096\n"
      "s_mov_b32 s80, 8736\n"
      "s_waitcnt vmcnt(0)\n"
      "s_mov_b64 exec, 1\n"
      "buffer_store_dwordx2 v[204:205], v4, s[76:79], s74 offen sc1\n"
      "s_mov_b64 exec, -1\n"
      "v_lshlrev_b32 v124, 16, v212\n"
      "v_and_b32 v125, s83, v212\n"
      "v_lshlrev_b32 v126, 16, v213\n"
      "v_and_b32 v127, s83, v213\n"
      "v_lshlrev_b32 v128, 16, v214\n"
      "v_and_b32 v129, s83, v214\n"
      "v_lshlrev_b32 v130, 16, v215\n"
      "v_and_b32 v131, s83, v215\n"
      "ds_write_b128 v210, v[212:215] offset:256\n"
      "v_lshlrev_b32 v140, 16, v220\n"
      "v_and_b32 v141, s83, v220\n"
      "v_lshlrev_b32 v142, 16, v221\n"
      "v_and_b32 v143, s83, v221\n"
      "v_lshlrev_b32 v144, 16, v222\n"
      "v_and_b32 v145, s83, v222\n"
      "v_lshlrev_b32 v146, 16, v223\n"
      "v_and_b32 v147, s83, v223\n"
      "v_pk_fma_f32 v[156:157], v[140:141], s[88:89], v[124:125] op_sel:[0,0,0] op_sel_hi:[1,0,1]\n"
      "v_pk_fma_f32 v[158:159], v[142:143], s[88:89], v[126:127] op_sel:[0,0,0] op_sel_hi:[1,0,1]\n"
      "v_pk_fma_f32 v[160:161], v[144:145], s[88:89], v[128:129] op_sel:[0,0,0] op_sel_hi:[1,0,1]\n"
      "v_pk_fma_f32 v[162:163], v[146:147], s[88:89], v[130:131] op_sel:[0,0,0] op_sel_hi:[1,0,1]\n"
      "v_pk_fma_f32 v[172:173], v[140:141], s[90:91], v[124:125] op_sel:[0,0,0] op_sel_hi:[1,0,1]\n"
      "v_pk_fma_f32 v[174:175], v[142:143], s[90:91], v[126:127] op_sel:[0,0,0] op_sel_hi:[1,0,1]\n"
      "v_pk_fma_f32 v[176:177], v[144:145], s[90:91], v[128:129] op_sel:[0,0,0] op_sel_hi:[1,0,1]\n"
      "v_pk_fma_f32 v[178:179], v[146:147], s[90:91], v[130:131] op_sel:[0,0,0] op_sel_hi:[1,0,1]\n"
      "v_lshlrev_b32 v148, 16, v224\n"
      "v_and_b32 v149, s83, v224\n"
      "v_lshlrev_b32 v150, 16, v225\n"
      "v_and_b32 v151, s83, v225\n"
      "v_lshlrev_b32 v152, 16, v226\n"
      "v_and_b32 v153, s83, v226\n"
      "v_lshlrev_b32 v154, 16, v227\n"
      "v_and_b32 v155, s83, v227\n"
      "ds_write_b128 v210, v[224:227] offset:320\n"
      "v_lshlrev_b32 v132, 16, v216\n"
      "v_and_b32 v133, s83, v216\n"
      "v_lshlrev_b32 v134, 16, v217\n"
      "v_and_b32 v135, s83, v217\n"
      "v_lshlrev_b32 v136, 16, v218\n"
      "v_and_b32 v137, s83, v218\n"
      "v_lshlrev_b32 v138, 16, v219\n"
      "v_and_b32 v139, s83, v219\n"
      "v_pk_fma_f32 v[164:165], v[148:149], s[88:89], v[132:133] op_sel:[0,0,0] op_sel_hi:[1,0,1]\n"
      "v_pk_fma_f32 v[166:167], v[150:151], s[88:89], v[134:135] op_sel:[0,0,0] op_sel_hi:[1,0,1]\n"
      "v_pk_fma_f32 v[168:169], v[152:153], s[88:89], v[136:137] op_sel:[0,0,0] op_sel_hi:[1,0,1]\n"
      "v_pk_fma_f32 v[170:171], v[154:155], s[88:89], v[138:139] op_sel:[0,0,0] op_sel_hi:[1,0,1]\n"
      "v_pk_fma_f32 v[188:189], v[164:165], s[88:89], v[156:157] op_sel:[0,1,0] op_sel_hi:[1,1,1]\n"
      "v_pk_fma_f32 v[190:191], v[166:167], s[88:89], v[158:159] op_sel:[0,1,0] op_sel_hi:[1,1,1]\n"
      "v_pk_fma_f32 v[192:193], v[168:169], s[88:89], v[160:161] op_sel:[0,1,0] op_sel_hi:[1,1,1]\n"
      "v_pk_fma_f32 v[194:195], v[170:171], s[88:89], v[162:163] op_sel:[0,1,0] op_sel_hi:[1,1,1]\n"
      "v_cvt_pk_bf16_f32 v228, v188, v189\n"
      "v_cvt_pk_bf16_f32 v229, v190, v191\n"
      "v_cvt_pk_bf16_f32 v230, v192, v193\n"
      "v_cvt_pk_bf16_f32 v231, v194, v195\n"
      "ds_write_b128 v210, v[228:231] offset:0\n"
      "v_pk_fma_f32 v[188:189], v[164:165], s[88:89], v[156:157] op_sel:[0,1,0] op_sel_hi:[1,1,1] neg_lo:[1,0,0] neg_hi:[1,0,0]\n"
      "v_pk_fma_f32 v[190:191], v[166:167], s[88:89], v[158:159] op_sel:[0,1,0] op_sel_hi:[1,1,1] neg_lo:[1,0,0] neg_hi:[1,0,0]\n"
      "v_pk_fma_f32 v[192:193], v[168:169], s[88:89], v[160:161] op_sel:[0,1,0] op_sel_hi:[1,1,1] neg_lo:[1,0,0] neg_hi:[1,0,0]\n"
      "v_pk_fma_f32 v[194:195], v[170:171], s[88:89], v[162:163] op_sel:[0,1,0] op_sel_hi:[1,1,1] neg_lo:[1,0,0] neg_hi:[1,0,0]\n"
      "v_cvt_pk_bf16_f32 v232, v188, v189\n"
      "v_cvt_pk_bf16_f32 v233, v190, v191\n"
      "v_cvt_pk_bf16_f32 v234, v192, v193\n"
      "v_cvt_pk_bf16_f32 v235, v194, v195\n"
      "ds_write_b128 v210, v[232:235] offset:64\n"
      "v_pk_fma_f32 v[180:181], v[148:149], s[90:91], v[132:133] op_sel:[0,0,0] op_sel_hi:[1,0,1]\n"
      "v_pk_fma_f32 v[182:183], v[150:151], s[90:91], v[134:135] op_sel:[0,0,0] op_sel_hi:[1,0,1]\n"
      "v_pk_fma_f32 v[184:185], v[152:153], s[90:91], v[136:137] op_sel:[0,0,0] op_sel_hi:[1,0,1]\n"
      "v_pk_fma_f32 v[186:187], v[154:155], s[90:91], v[138:139] op_sel:[0,0,0] op_sel_hi:[1,0,1]\n"
      "v_pk_fma_f32 v[188:189], v[180:181], s[90:91], v[172:173] op_sel:[0,1,0] op_sel_hi:[1,1,1] neg_lo:[1,0,0] neg_hi:[1,0,0]\n"
      "v_pk_fma_f32 v[190:191], v[182:183], s[90:91], v[174:175] op_sel:[0,1,0] op_sel_hi:[1,1,1] neg_lo:[1,0,0] neg_hi:[1,0,0]\n"
      "v_pk_fma_f32 v[192:193], v[184:185], s[90:91], v[176:177] op_sel:[0,1,0] op_sel_hi:[1,1,1] neg_lo:[1,0,0] neg_hi:[1,0,0]\n"
      "v_pk_fma_f32 v[194:195], v[186:187], s[90:91], v[178:179] op_sel:[0,1,0] op_sel_hi:[1,1,1] neg_lo:[1,0,0] neg_hi:[1,0,0]\n"
      "v_cvt_pk_bf16_f32 v240, v188, v189\n"
      "v_cvt_pk_bf16_f32 v241, v190, v191\n"
      "v_cvt_pk_bf16_f32 v242, v192, v193\n"
      "v_cvt_pk_bf16_f32 v243, v194, v195\n"
      "ds_write_b128 v210, v[240:243] offset:192\n"
      "v_pk_fma_f32 v[196:197], v[180:181], s[90:91], v[172:173] op_sel:[0,1,0] op_sel_hi:[1,1,1]\n"
      "v_pk_fma_f32 v[198:199], v[182:183], s[90:91], v[174:175] op_sel:[0,1,0] op_sel_hi:[1,1,1]\n"
      "v_pk_fma_f32 v[200:201], v[184:185], s[90:91], v[176:177] op_sel:[0,1,0] op_sel_hi:[1,1,1]\n"
      "v_pk_fma_f32 v[202:203], v[186:187], s[90:91], v[178:179] op_sel:[0,1,0] op_sel_hi:[1,1,1]\n"
      "v_cvt_pk_bf16_f32 v236, v196, v197\n"
      "v_cvt_pk_bf16_f32 v237, v198, v199\n"
      "v_cvt_pk_bf16_f32 v238, v200, v201\n"
      "v_cvt_pk_bf16_f32 v239, v202, v203\n"
      "ds_write_b128 v210, v[236:239] offset:128\n"
      "v_pk_fma_f32 v[196:197], v[164:165], s[92:93], v[196:197] op_sel:[0,0,0] op_sel_hi:[1,0,1]\n"
      "v_pk_fma_f32 v[198:199], v[166:167], s[92:93], v[198:199] op_sel:[0,0,0] op_sel_hi:[1,0,1]\n"
      "v_pk_fma_f32 v[200:201], v[168:169], s[92:93], v[200:201] op_sel:[0,0,0] op_sel_hi:[1,0,1]\n"
      "v_pk_fma_f32 v[202:203], v[170:171], s[92:93], v[202:203] op_sel:[0,0,0] op_sel_hi:[1,0,1]\n"
      "v_pk_fma_f32 v[196:197], v[124:125], s[92:93], v[196:197] op_sel:[0,1,0] op_sel_hi:[1,1,1]\n"
      "v_pk_fma_f32 v[198:199], v[126:127], s[92:93], v[198:199] op_sel:[0,1,0] op_sel_hi:[1,1,1]\n"
      "v_pk_fma_f32 v[200:201], v[128:129], s[92:93], v[200:201] op_sel:[0,1,0] op_sel_hi:[1,1,1]\n"
      "v_pk_fma_f32 v[202:203], v[130:131], s[92:93], v[202:203] op_sel:[0,1,0] op_sel_hi:[1,1,1]\n"
      "v_cvt_pk_bf16_f32 v252, v196, v197\n"
      "v_cvt_pk_bf16_f32 v253, v198, v199\n"
      "v_cvt_pk_bf16_f32 v254, v200, v201\n"
      "v_cvt_pk_bf16_f32 v255, v202, v203\n"
      "ds_write_b128 v210, v[252:255] offset:384\n"
      "s_waitcnt lgkmcnt(0)\n"
      "v_mov_b32 v228, v204\n"
      "v_mov_b32 v229, v205\n"
      "v_and_b32 v232, 31, v7\n"
      "v_lshlrev_b32 v232, 3, v232\n"
      "s_sleep 18\n"
      "LPOLL%=:\n"
      "buffer_load_dwordx2 v[208:209], v206, s[76:79], 0 offen sc1\n"
      "s_sub_u32 s71, s71, 1\n"
      "s_waitcnt vmcnt(0)\n"
      "v_xor_b32 v208, v208, v204\n"
      "v_xor_b32 v209, v209, v205\n"
      "v_or_b32 v208, v208, v209\n"
      "v_cmp_ne_u32 vcc, 0, v208\n"
      "s_cmp_eq_u32 s71, 0\n"
      "s_cbranch_scc1 LPDONE%=\n"
      "s_and_b32 s70, vcc_lo, 0xff\n"
      "s_cbranch_scc1 LPOLL%=\n"
      "LPDONE%=:\n"
      "ds_write_b64 v207, v[204:205]\n"
      "s_branch LSPINX%=\n"
      "LNURG%=:\n"
      "v_perm_b32 v148, v148, v148, s84\n"
      "v_perm_b32 v149, v149, v149, s84\n"
      "v_perm_b32 v150, v150, v150, s84\n"
      "v_perm_b32 v151, v151, v151, s84\n"
      "v_perm_b32 v152, v152, v152, s84\n"
      "v_perm_b32 v153, v153, v153, s84\n"
      "v_perm_b32 v154, v154, v154, s84\n"
      "v_perm_b32 v155, v155, v155, s84\n"
      "v_cvt_pk_bf16_f32 v244, v148, v149\n"
      "v_cvt_pk_bf16_f32 v245, v150, v151\n"
      "v_cvt_pk_bf16_f32 v246, v152, v153\n"
      "v_cvt_pk_bf16_f32 v247, v154, v155\n"
      "v_perm_b32 v132, v132, v132, s84\n"
      "v_perm_b32 v133, v133, v133, s84\n"
      "v_perm_b32 v134, v134, v134, s84\n"
      "v_perm_b32 v135, v135, v135, s84\n"
      "v_perm_b32 v136, v136, v136, s84\n"
      "v_perm_b32 v137, v137, v137, s84\n"
      "v_perm_b32 v138, v138, v138, s84\n"
      "v_perm_b32 v139, v139, v139, s84\n"
      "v_pk_fma_f32 v[156:157], v[132:133], s[88:89], v[148:149] op_sel:[0,0,0] op_sel_hi:[1,0,1]\n"
      "v_pk_fma_f32 v[158:159], v[134:135], s[88:89], v[150:151] op_sel:[0,0,0] op_sel_hi:[1,0,1]\n"
      "v_pk_fma_f32 v[160:161], v[136:137], s[88:89], v[152:153] op_sel:[0,0,0] op_sel_hi:[1,0,1]\n"
      "v_pk_fma_f32 v[162:163], v[138:139], s[88:89], v[154:155] op_sel:[0,0,0] op_sel_hi:[1,0,1]\n"
      "v_pk_fma_f32 v[172:173], v[132:133], s[90:91], v[148:149] op_sel:[0,0,0] op_sel_hi:[1,0,1]\n"
      "v_pk_fma_f32 v[174:175], v[134:135], s[90:91], v[150:151] op_sel:[0,0,0] op_sel_hi:[1,0,1]\n"
      "v_pk_fma_f32 v[176:177], v[136:137], s[90:91], v[152:153] op_sel:[0,0,0] op_sel_hi:[1,0,1]\n"
      "v_pk_fma_f32 v[178:179], v[138:139], s[90:91], v[154:155] op_sel:[0,0,0] op_sel_hi:[1,0,1]\n"
      "v_perm_b32 v124, v124, v124, s84\n"
      "v_perm_b32 v125, v125, v125, s84\n"
      "v_perm_b32 v126, v126, v126, s84\n"
      "v_perm_b32 v127, v127, v127, s84\n"
      "v_perm_b32 v128, v128, v128, s84\n"
      "v_perm_b32 v129, v129, v129, s84\n"
      "v_perm_b32 v130, v130, v130, s84\n"
      "v_perm_b32 v131, v131, v131, s84\n"
      "v_cvt_pk_bf16_f32 v248, v124, v125\n"
      "v_cvt_pk_bf16_f32 v249, v126, v127\n"
      "v_cvt_pk_bf16_f32 v250, v128, v129\n"
      "v_cvt_pk_bf16_f32 v251, v130, v131\n"
      "v_perm_b32 v140, v140, v140, s84\n"
      "v_perm_b32 v141, v141, v141, s84\n"
      "v_perm_b32 v142, v142, v142, s84\n"
      "v_perm_b32 v143, v143, v143, s84\n"
      "v_perm_b32 v144, v144, v144, s84\n"
      "v_perm_b32 v145, v145, v145, s84\n"
      "v_perm_b32 v146, v146, v146, s84\n"
      "v_perm_b32 v147, v147, v147, s84\n"
      "v_pk_fma_f32 v[164:165], v[124:125], s[88:89], v[140:141] op_sel:[0,0,0] op_sel_hi:[1,0,1]\n"
      "v_pk_fma_f32 v[166:167], v[126:127], s[88:89], v[142:143] op_sel:[0,0,0] op_sel_hi:[1,0,1]\n"
      "v_pk_fma_f32 v[168:169], v[128:129], s[88:89], v[144:145] op_sel:[0,0,0] op_sel_hi:[1,0,1]\n"
      "v_pk_fma_f32 v[170:171], v[130:131], s[88:89], v[146:147] op_sel:[0,0,0] op_sel_hi:[1,0,1]\n"
      "v_pk_fma_f32 v[188:189], v[164:165], s[88:89], v[156:157] op_sel:[0,1,0] op_sel_hi:[1,1,1]\n"
      "v_pk_fma_f32 v[190:191], v[166:167], s[88:89], v[158:159] op_sel:[0,1,0] op_sel_hi:[1,1,1]\n"
      "v_pk_fma_f32 v[192:193], v[168:169], s[88:89], v[160:161] op_sel:[0,1,0] op_sel_hi:[1,1,1]\n"
      "v_pk_fma_f32 v[194:195], v[170:171], s[88:89], v[162:163] op_sel:[0,1,0] op_sel_hi:[1,1,1]\n"
      "v_cvt_pk_bf16_f32 v228, v188, v189\n"
      "v_cvt_pk_bf16_f32 v229, v190, v191\n"
      "v_cvt_pk_bf16_f32 v230, v192, v193\n"
      "v_cvt_pk_bf16_f32 v231, v194, v195\n"
      "v_pk_fma_f32 v[188:189], v[164:165], s[88:89], v[156:157] op_sel:[0,1,0] op_sel_hi:[1,1,1] neg_lo:[1,0,0] neg_hi:[1,0,0]\n"
      "v_pk_fma_f32 v[190:191], v[166:167], s[88:89], v[158:159] op_sel:[0,1,0] op_sel_hi:[1,1,1] neg_lo:[1,0,0] neg_hi:[1,0,0]\n"
      "v_pk_fma_f32 v[192:193], v[168:169], s[88:89], v[160:161] op_sel:[0,1,0] op_sel_hi:[1,1,1] neg_lo:[1,0,0] neg_hi:[1,0,0]\n"
      "v_pk_fma_f32 v[194:195], v[170:171], s[88:89], v[162:163] op_sel:[0,1,0] op_sel_hi:[1,1,1] neg_lo:[1,0,0] neg_hi:[1,0,0]\n"
      "v_cvt_pk_bf16_f32 v232, v188, v189\n"
      "v_cvt_pk_bf16_f32 v233, v190, v191\n"
      "v_cvt_pk_bf16_f32 v234, v192, v193\n"
      "v_cvt_pk_bf16_f32 v235, v194, v195\n"
      "v_pk_fma_f32 v[180:181], v[124:125], s[90:91], v[140:141] op_sel:[0,0,0] op_sel_hi:[1,0,1]\n"
      "v_pk_fma_f32 v[182:183], v[126:127], s[90:91], v[142:143] op_sel:[0,0,0] op_sel_hi:[1,0,1]\n"
      "v_pk_fma_f32 v[184:185], v[128:129], s[90:91], v[144:145] op_sel:[0,0,0] op_sel_hi:[1,0,1]\n"
      "v_pk_fma_f32 v[186:187], v[130:131], s[90:91], v[146:147] op_sel:[0,0,0] op_sel_hi:[1,0,1]\n"
      "v_pk_fma_f32 v[188:189], v[180:181], s[90:91], v[172:173] op_sel:[0,1,0] op_sel_hi:[1,1,1] neg_lo:[1,0,0] neg_hi:[1,0,0]\n"
      "v_pk_fma_f32 v[190:191], v[182:183], s[90:91], v[174:175] op_sel:[0,1,0] op_sel_hi:[1,1,1] neg_lo:[1,0,0] neg_hi:[1,0,0]\n"
      "v_pk_fma_f32 v[192:193], v[184:185], s[90:91], v[176:177] op_sel:[0,1,0] op_sel_hi:[1,1,1] neg_lo:[1,0,0] neg_hi:[1,0,0]\n"
      "v_pk_fma_f32 v[194:195], v[186:187], s[90:91], v[178:179] op_sel:[0,1,0] op_sel_hi:[1,1,1] neg_lo:[1,0,0] neg_hi:[1,0,0]\n"
      "v_cvt_pk_bf16_f32 v240, v188, v189\n"
      "v_cvt_pk_bf16_f32 v241, v190, v191\n"
      "v_cvt_pk_bf16_f32 v242, v192, v193\n"
      "v_cvt_pk_bf16_f32 v243, v194, v195\n"
      "v_pk_fma_f32 v[196:197], v[180:181], s[90:91], v[172:173] op_sel:[0,1,0] op_sel_hi:[1,1,1]\n"
      "v_pk_fma_f32 v[198:199], v[182:183], s[90:91], v[174:175] op_sel:[0,1,0] op_sel_hi:[1,1,1]\n"
      "v_pk_fma_f32 v[200:201], v[184:185], s[90:91], v[176:177] op_sel:[0,1,0] op_sel_hi:[1,1,1]\n"
      "v_pk_fma_f32 v[202:203], v[186:187], s[90:91], v[178:179] op_sel:[0,1,0] op_sel_hi:[1,1,1]\n"
      "v_cvt_pk_bf16_f32 v236, v196, v197\n"
      "v_cvt_pk_bf16_f32 v237, v198, v199\n"
      "v_cvt_pk_bf16_f32 v238, v200, v201\n"
      "v_cvt_pk_bf16_f32 v239, v202, v203\n"
      "v_pk_fma_f32 v[196:197], v[164:165], s[92:93], v[196:197] op_sel:[0,0,0] op_sel_hi:[1,0,1]\n"
      "v_pk_fma_f32 v[198:199], v[166:167], s[92:93], v[198:199] op_sel:[0,0,0] op_sel_hi:[1,0,1]\n"
      "v_pk_fma_f32 v[200:201], v[168:169], s[92:93], v[200:201] op_sel:[0,0,0] op_sel_hi:[1,0,1]\n"
      "v_pk_fma_f32 v[202:203], v[170:171], s[92:93], v[202:203] op_sel:[0,0,0] op_sel_hi:[1,0,1]\n"
      "v_pk_fma_f32 v[196:197], v[148:149], s[92:93], v[196:197] op_sel:[0,1,0] op_sel_hi:[1,1,1]\n"
      "v_pk_fma_f32 v[198:199], v[150:151], s[92:93], v[198:199] op_sel:[0,1,0] op_sel_hi:[1,1,1]\n"
      "v_pk_fma_f32 v[200:201], v[152:153], s[92:93], v[200:201] op_sel:[0,1,0] op_sel_hi:[1,1,1]\n"
      "v_pk_fma_f32 v[202:203], v[154:155], s[92:93], v[202:203] op_sel:[0,1,0] op_sel_hi:[1,1,1]\n"
      "v_cvt_pk_bf16_f32 v252, v196, v197\n"
      "v_cvt_pk_bf16_f32 v253, v198, v199\n"
      "v_cvt_pk_bf16_f32 v254, v200, v201\n"
      "v_cvt_pk_bf16_f32 v255, v202, v203\n"
      "LSPIN%=:\n"
      "ds_read_b64 v[208:209], v207\n"
      "s_waitcnt lgkmcnt(0)\n"
      "v_xor_b32 v208, v208, v204\n"
      "v_xor_b32 v209, v209, v205\n"
      "v_or_b32 v208, v208, v209\n"
      "v_cmp_ne_u32 vcc, 0, v208\n"
      "s_sleep 1\n"
      "s_nop 1\n"
      "s_cbranch_vccnz LSPIN%=\n"
      "LSPINX%=:\n"
      "s_waitcnt vmcnt(0) lgkmcnt(0)\n"
      "buffer_load_dwordx4 v[124:127], v4, s[44:47], s49 offen offset:0 sc1\n"
      "buffer_load_dwordx4 v[128:131], v4, s[44:47], s49 offen offset:1024 sc1\n"
      "buffer_load_dwordx4 v[132:135], v4, s[44:47], s49 offen offset:2048 sc1\n"
      "buffer_load_dwordx4 v[136:139], v4, s[44:47], s49 offen offset:3072 sc1\n"
      "buffer_load_dwordx4 v[140:143], v4, s[44:47], s50 offen offset:0 sc1\n"
      "buffer_load_dwordx4 v[144:147], v4, s[44:47], s50 offen offset:1024 sc1\n"
      "buffer_load_dwordx4 v[148:151], v4, s[44:47], s50 offen offset:2048 sc1\n"
      "buffer_load_dwordx4 v[152:155], v4, s[44:47], s50 offen offset:3072 sc1\n"
      "buffer_load_dwordx4 v[156:159], v4, s[44:47], s51 offen offset:0 sc1\n"
      "buffer_load_dwordx4 v[160:163], v4, s[44:47], s51 offen offset:1024 sc1\n"
      "buffer_load_dwordx4 v[164:167], v4, s[44:47], s51 offen offset:2048 sc1\n"
      "buffer_load_dwordx4 v[168:171], v4, s[44:47], s51 offen offset:3072 sc1\n"
      "s_add_u32 s49, s49, 57344\n"
      "s_add_u32 s50, s50, 57344\n"
      "s_cmp_lg_u32 %[wv], 1\n"
      "s_cbranch_scc1 LPB2%=\n"
      "LPB%=:\n"
      "v_cmp_ne_u32 vcc, 0, v208\n"
      "s_sub_u32 s71, s71, 1\n"
      "s_cmp_le_i32 s71, 0\n"
      "s_cbranch_scc1 LPB2%=\n"
      "s_nop 1\n"
      "s_cbranch_vccz LPB2%=\n"
      "buffer_load_dwordx2 v[208:209], v206, s[76:79], 0 offen sc1\n"
      "s_waitcnt vmcnt(0)\n"
      "v_xor_b32 v208, v208, v204\n"
      "v_xor_b32 v209, v209, v205\n"
      "v_or_b32 v208, v208, v209\n"
      "s_branch LPB%=\n"
      "LPB2%=:\n"
      "s_barrier\n"
      "s_buffer_load_dwordx2 s[86:87], s[40:43], s81 offset:0\n"
      "s_buffer_load_dwordx2 s[86:87], s[40:43], s81 offset:1024\n"
      "s_add_u32 s81, s81, s82\n"
      "s_sub_u32 s82, 128, s82\n"
      "ds_read_b128 v[188:191], v0 offset:0\n"
      "ds_read_b128 v[192:195], v0 offset:1024\n"
      "ds_read_b128 v[196:199], v0 offset:2048\n"
      "ds_read_b128 v[200:203], v0 offset:3072\n"
      "ds_read_b128 v[204:207], v0 offset:4096\n"
      "ds_read_b128 v[208:211], v0 offset:5120\n"
      "ds_read_b128 v[216:219], v0 offset:6144\n"
      "ds_read_b128 v[220:223], v0 offset:7168\n"
      "ds_read_b128 v[224:227], v1 offset:0\n"
      "v_add_u32 v0, s54, v0\n"
      "s_waitcnt vmcnt(11) lgkmcnt(8)\n"
      ".p2align 6\n"
      "LCL%=:\n"
      "v_mfma_f32_16x16x32_bf16 v[12:15], v[124:127], v[188:191], v[12:15]\n"
      "s_add_u32 s51, s51, 57344\n"
      "s_cmp_eq_u32 s55, s75\n"
      "s_cselect_b64 vcc, -1, 0\n"
      "s_waitcnt vmcnt(9) lgkmcnt(7)\n"
      "v_mfma_f32_16x16x32_bf16 v[16:19], v[128:131], v[188:191], v[16:19]\n"
      "ds_read_b128 v[188:191], v1 offset:1024\n"
      "v_mfma_f32_16x16x32_bf16 v[20:23], v[124:127], v[192:195], v[20:23]\n"
      "v_mfma_f32_16x16x32_bf16 v[24:27], v[128:131], v[192:195], v[24:27]\n"
      "s_cbranch_vccz LST0%=\n"
      "s_add_u32 s70, s68, 0\n"
      "buffer_store_dwordx4 v[228:231], v4, s[44:47], s70 offen sc1\n"
      "LST0%=:\n"
      "v_mfma_f32_16x16x32_bf16 v[12:15], v[132:135], v[192:195], v[12:15]\n"
      "s_waitcnt vmcnt(7) lgkmcnt(7)\n"
      "v_mfma_f32_16x16x32_bf16 v[16:19], v[136:139], v[192:195], v[16:19]\n"
      "ds_read_b128 v[192:195], v1 offset:2048\n"
      "v_mfma_f32_16x16x32_bf16 v[28:31], v[124:127], v[196:199], v[28:31]\n"
      "v_add_u32 v1, v8, v1\n"
      "v_mfma_f32_16x16x32_bf16 v[32:35], v[128:131], v[196:199], v[32:35]\n"
      "buffer_load_dwordx4 v[172:175], v4, s[44:47], s52 offen offset:0 sc1\n"
      "v_mfma_f32_16x16x32_bf16 v[20:23], v[132:135], v[196:199], v[20:23]\n"
      "v_mfma_f32_16x16x32_bf16 v[24:27], v[136:139], v[196:199], v[24:27]\n"
      "v_mfma_f32_16x16x32_bf16 v[12:15], v[140:143], v[196:199], v[12:15]\n"
      "s_waitcnt vmcnt(6) lgkmcnt(7)\n"
      "v_mfma_f32_16x16x32_bf16 v[16:19], v[144:147], v[196:199], v[16:19]\n"
      "ds_read_b128 v[196:199], v2 offset:0\n"
      "v_mfma_f32_16x16x32_bf16 v[36:39], v[124:127], v[200:203], v[36:39]\n"
      "buffer_load_dwordx4 v[176:179], v4, s[44:47], s52 offen offset:1024 sc1\n"
      "v_mfma_f32_16x16x32_bf16 v[40:43], v[128:131], v[200:203], v[40:43]\n"
      "v_mfma_f32_16x16x32_bf16 v[28:31], v[132:135], v[200:203], v[28:31]\n"
      "s_cbranch_vccz LST1%=\n"
      "s_add_u32 s70, s68, 8192\n"
      "buffer_store_dwordx4 v[232:235], v4, s[44:47], s70 offen sc1\n"
      "LST1%=:\n"
      "v_mfma_f32_16x16x32_bf16 v[32:35], v[136:139], v[200:203], v[32:35]\n"
      "v_mfma_f32_16x16x32_bf16 v[20:23], v[140:143], v[200:203], v[20:23]\n"
      "buffer_load_dwordx4 v[180:183], v4, s[44:47], s52 offen offset:2048 sc1\n"
      "v_mfma_f32_16x16x32_bf16 v[24:27], v[144:147], v[200:203], v[24:27]\n"
      "v_mfma_f32_16x16x32_bf16 v[12:15], v[148:151], v[200:203], v[12:15]\n"
      "s_waitcnt vmcnt(7) lgkmcnt(7)\n"
      "v_mfma_f32_16x16x32_bf16 v[16:19], v[152:155], v[200:203], v[16:19]\n"
      "ds_read_b128 v[200:203], v2 offset:1024\n"
      "v_mfma_f32_16x16x32_bf16 v[44:47], v[124:127], v[204:207], v[44:47]\n"
      "v_mfma_f32_16x16x32_bf16 v[48:51], v[128:131], v[204:207], v[48:51]\n"
      "s_cbranch_vccz LST2%=\n"
      "s_add_u32 s70, s68, 16384\n"
      "buffer_store_dwordx4 v[236:239], v4, s[44:47], s70 offen sc1\n"
      "LST2%=:\n"
      "v_mfma_f32_16x16x32_bf16 v[36:39], v[132:135], v[204:207], v[36:39]\n"
      "buffer_load_dwordx4 v[184:187], v4, s[44:47], s52 offen offset:3072 sc1\n"
      "v_mfma_f32_16x16x32_bf16 v[40:43], v[136:139], v[204:207], v[40:43]\n"
      "s_add_u32 s52, s52, 57344\n"
      "v_mfma_f32_16x16x32_bf16 v[28:31], v[140:143], v[204:207], v[28:31]\n"
      "v_mfma_f32_16x16x32_bf16 v[32:35], v[144:147], v[204:207], v[32:35]\n"
      "v_mfma_f32_16x16x32_bf16 v[20:23], v[148:151], v[204:207], v[20:23]\n"
      "s_waitcnt lgkmcnt(7)\n"
      "v_mfma_f32_16x16x32_bf16 v[24:27], v[152:155], v[204:207], v[24:27]\n"
      "ds_read_b128 v[204:207], v2 offset:2048\n"
      "v_mfma_f32_16x16x32_bf16 v[52:55], v[124:127], v[208:211], v[52:55]\n"
      "v_mfma_f32_16x16x32_bf16 v[56:59], v[128:131], v[208:211], v[56:59]\n"
      "s_cbranch_vccz LST3%=\n"
      "s_add_u32 s70, s68, 24576\n"
      "buffer_store_dwordx4 v[240:243], v4, s[44:47], s70 offen sc1\n"
      "LST3%=:\n"
      "v_mfma_f32_16x16x32_bf16 v[44:47], v[132:135], v[208:211], v[44:47]\n"
      "v_mfma_f32_16x16x32_bf16 v[48:51], v[136:139], v[208:211], v[48:51]\n"
      "v_mfma_f32_16x16x32_bf16 v[36:39], v[140:143], v[208:211], v[36:39]\n"
      "v_mfma_f32_16x16x32_bf16 v[40:43], v[144:147], v[208:211], v[40:43]\n"
      "s_cbranch_vccz LST4%=\n"
      "s_add_u32 s70, s69, 32768\n"
      "buffer_store_dwordx4 v[244:247], v4, s[44:47], s70 offen sc1\n"
      "LST4%=:\n"
      "v_mfma_f32_16x16x32_bf16 v[28:31], v[148:151], v[208:211], v[28:31]\n"
      "s_waitcnt lgkmcnt(7)\n"
      "v_mfma_f32_16x16x32_bf16 v[32:35], v[152:155], v[208:211], v[32:35]\n"
      "ds_read_b128 v[208:211], v2 offset:3072\n"
      "v_mfma_f32_16x16x32_bf16 v[60:63], v[124:127], v[216:219], v[60:63]\n"
      "ds_read_b128 v[212:215], v2 offset:4096\n"
      "v_mfma_f32_16x16x32_bf16 v[64:67], v[128:131], v[216:219], v[64:67]\n"
      "v_mfma_f32_16x16x32_bf16 v[52:55], v[132:135], v[216:219], v[52:55]\n"
      "v_mfma_f32_16x16x32_bf16 v[56:59], v[136:139], v[216:219], v[56:59]\n"
      "v_mfma_f32_16x16x32_bf16 v[44:47], v[140:143], v[216:219], v[44:47]\n"
      "s_cbranch_vccz LST5%=\n"
      "s_add_u32 s70, s69, 40960\n"
      "buffer_store_dwordx4 v[248:251], v4, s[44:47], s70 offen sc1\n"
      "LST5%=:\n"
      "v_mfma_f32_16x16x32_bf16 v[48:51], v[144:147], v[216:219], v[48:51]\n"
      "s_cmp_lg_u32 s55, s73\n"
      "s_cbranch_scc1 LFL%=\n"
      "s_waitcnt vmcnt(0)\n"
      "v_mov_b32 v228, %[tag]\n"
      "v_not_b32 v229, v228\n"
      "s_mov_b64 exec, 1\n"
      "buffer_store_dwordx2 v[228:229], v4, s[76:79], s74 offen sc1\n"
      "s_mov_b64 exec, -1\n"
      "LFL%=:\n"
      "v_mfma_f32_16x16x32_bf16 v[36:39], v[148:151], v[216:219], v[36:39]\n"
      "s_waitcnt lgkmcnt(8)\n"
      "v_mfma_f32_16x16x32_bf16 v[40:43], v[152:155], v[216:219], v[40:43]\n"
      "ds_read_b128 v[216:219], v2 offset:5120\n"
      "v_mfma_f32_16x16x32_bf16 v[68:71], v[124:127], v[220:223], v[68:71]\n"
      "buffer_load_dwordx4 v[124:127], v4, s[44:47], s49 offen offset:0 sc1\n"
      "v_mfma_f32_16x16x32_bf16 v[72:75], v[128:131], v[220:223], v[72:75]\n"
      "v_mfma_f32_16x16x32_bf16 v[60:63], v[132:135], v[220:223], v[60:63]\n"
      "v_mfma_f32_16x16x32_bf16 v[64:67], v[136:139], v[220:223], v[64:67]\n"
      "s_cbranch_vccz LST6%=\n"
      "s_add_u32 s70, s69, 49152\n"
      "buffer_store_dwordx4 v[252:255], v4, s[44:47], s70 offen sc1\n"
      "LST6%=:\n"
      "v_mfma_f32_16x16x32_bf16 v[52:55], v[140:143], v[220:223], v[52:55]\n"
      "v_mfma_f32_16x16x32_bf16 v[56:59], v[144:147], v[220:223], v[56:59]\n"
      "v_mfma_f32_16x16x32_bf16 v[44:47], v[148:151], v[220:223], v[44:47]\n"
      "s_waitcnt lgkmcnt(8)\n"
      "v_mfma_f32_16x16x32_bf16 v[48:51], v[152:155], v[220:223], v[48:51]\n"
      "ds_read_b128 v[220:223], v2 offset:6144\n"
      "v_mfma_f32_16x16x32_bf16 v[68:71], v[132:135], v[224:227], v[68:71]\n"
      "buffer_load_dwordx4 v[128:131], v4, s[44:47], s49 offen offset:1024 sc1\n"
      "v_mfma_f32_16x16x32_bf16 v[72:75], v[136:139], v[224:227], v[72:75]\n"
      "v_mfma_f32_16x16x32_bf16 v[60:63], v[140:143], v[224:227], v[60:63]\n"
      "v_mfma_f32_16x16x32_bf16 v[64:67], v[144:147], v[224:227], v[64:67]\n"
      "s_bitcmp1_b32 s80, s55\n"
      "s_cbranch_scc0 LPL1%=\n"
      "buffer_load_dwordx2 v[230:231], v232, s[76:79], s72 offen sc1\n"
      "LPL1%=:\n"
      "v_mfma_f32_16x16x32_bf16 v[52:55], v[148:151], v[224:227], v[52:55]\n"
      "s_waitcnt lgkmcnt(8)\n"
      "v_mfma_f32_16x16x32_bf16 v[56:59], v[152:155], v[224:227], v[56:59]\n"
      "ds_read_b128 v[224:227], v2 offset:7168\n"
      "v_mfma_f32_16x16x32_bf16 v[68:71], v[140:143], v[188:191], v[68:71]\n"
      "v_add_u32 v2, s54, v2\n"
      "v_mfma_f32_16x16x32_bf16 v[72:75], v[144:147], v[188:191], v[72:75]\n"
      "buffer_load_dwordx4 v[132:135], v4, s[44:47], s49 offen offset:2048 sc1\n"
      "v_mfma_f32_16x16x32_bf16 v[60:63], v[148:151], v[188:191], v[60:63]\n"
      "s_waitcnt lgkmcnt(8)\n"
      "v_mfma_f32_16x16x32_bf16 v[64:67], v[152:155], v[188:191], v[64:67]\n"
      "ds_read_b128 v[188:191], v3 offset:0\n"
      "v_mfma_f32_16x16x32_bf16 v[68:71], v[148:151], v[192:195], v[68:71]\n"
      "s_waitcnt vmcnt(10) lgkmcnt(8)\n"
      "v_mfma_f32_16x16x32_bf16 v[72:75], v[152:155], v[192:195], v[72:75]\n"
      "ds_read_b128 v[192:195], v3 offset:1024\n"
      "v_mfma_f32_16x16x32_bf16 v[76:79], v[156:159], v[196:199], v[76:79]\n"
      "ds_read_b128 v[196:199], v3 offset:2048\n"
      "s_waitcnt lgkmcnt(9)\n"
      "v_mfma_f32_16x16x32_bf16 v[80:83], v[156:159], v[200:203], v[80:83]\n"
      "v_add_u32 v3, v8, v3\n"
      "s_waitcnt vmcnt(9) lgkmcnt(8)\n"
      "v_mfma_f32_16x16x32_bf16 v[76:79], v[160:163], v[200:203], v[76:79]\n"
      "ds_read_b128 v[200:203], v5 offset:0\n"
      "v_sub_u32 v8, v9, v8\n"
      "v_mfma_f32_16x16x32_bf16 v[84:87], v[156:159], v[204:207], v[84:87]\n"
      "buffer_load_dwordx4 v[136:139], v4, s[44:47], s49 offen offset:3072 sc1\n"
      "v_mfma_f32_16x16x32_bf16 v[80:83], v[160:163], v[204:207], v[80:83]\n"
      "buffer_load_dwordx4 v[140:143], v4, s[44:47], s50 offen offset:0 sc1\n"
      "s_waitcnt vmcnt(10) lgkmcnt(8)\n"
      "v_mfma_f32_16x16x32_bf16 v[76:79], v[164:167], v[204:207], v[76:79]\n"
      "ds_read_b128 v[204:207], v5 offset:1024\n"
      "s_add_u32 s49, s49, 57344\n"
      "v_mfma_f32_16x16x32_bf16 v[88:91], v[156:159], v[208:211], v[88:91]\n"
      "v_mfma_f32_16x16x32_bf16 v[84:87], v[160:163], v[208:211], v[84:87]\n"
      "v_mfma_f32_16x16x32_bf16 v[80:83], v[164:167], v[208:211], v[80:83]\n"
      "s_waitcnt vmcnt(9) lgkmcnt(8)\n"
      "v_mfma_f32_16x16x32_bf16 v[76:79], v[168:171], v[208:211], v[76:79]\n"
      "ds_read_b128 v[208:211], v5 offset:2048\n"
      "v_mfma_f32_16x16x32_bf16 v[92:95], v[156:159], v[212:215], v[92:95]\n"
      "buffer_load_dwordx4 v[144:147], v4, s[44:47], s50 offen offset:1024 sc1\n"
      "v_mfma_f32_16x16x32_bf16 v[88:91], v[160:163], v[212:215], v[88:91]\n"
      "v_mfma_f32_16x16x32_bf16 v[84:87], v[164:167], v[212:215], v[84:87]\n"
      "s_waitcnt lgkmcnt(8)\n"
      "v_mfma_f32_16x16x32_bf16 v[80:83], v[168:171], v[212:215], v[80:83]\n"
      "ds_read_b128 v[212:215], v5 offset:3072\n"
      "v_mfma_f32_16x16x32_bf16 v[96:99], v[156:159], v[216:219], v[96:99]\n"
      "v_add_u32 v5, s54, v5\n"
      "v_mfma_f32_16x16x32_bf16 v[92:95], v[160:163], v[216:219], v[92:95]\n"
      "s_sub_u32 s54, 0, s54\n"
      "v_mfma_f32_16x16x32_bf16 v[88:91], v[164:167], v[216:219], v[88:91]\n"
      "buffer_load_dwordx4 v[148:151], v4, s[44:47], s50 offen offset:2048 sc1\n"
      "s_waitcnt lgkmcnt(8)\n"
      "v_mfma_f32_16x16x32_bf16 v[84:87], v[168:171], v[216:219], v[84:87]\n"
      "ds_read_b128 v[216:219], v6 offset:0\n"
      "v_mfma_f32_16x16x32_bf16 v[100:103], v[156:159], v[220:223], v[100:103]\n"
      "v_mfma_f32_16x16x32_bf16 v[96:99], v[160:163], v[220:223], v[96:99]\n"
      "v_mfma_f32_16x16x32_bf16 v[92:95], v[164:167], v[220:223], v[92:95]\n"
      "s_waitcnt lgkmcnt(8)\n"
      "v_mfma_f32_16x16x32_bf16 v[88:91], v[168:171], v[220:223], v[88:91]\n"
      "ds_read_b128 v[220:223], v6 offset:1024\n"
      "v_mfma_f32_16x16x32_bf16 v[104:107], v[156:159], v[224:227], v[104:107]\n"
      "buffer_load_dwordx4 v[152:155], v4, s[44:47], s50 offen offset:3072 sc1\n"
      "v_mfma_f32_16x16x32_bf16 v[100:103], v[160:163], v[224:227], v[100:103]\n"
      "s_add_u32 s50, s50, 57344\n"
      "v_mfma_f32_16x16x32_bf16 v[96:99], v[164:167], v[224:227], v[96:99]\n"
      "s_waitcnt lgkmcnt(8)\n"
      "v_mfma_f32_16x16x32_bf16 v[92:95], v[168:171], v[224:227], v[92:95]\n"
      "ds_read_b128 v[224:227], v6 offset:2048\n"
      "v_mfma_f32_16x16x32_bf16 v[104:107], v[160:163], v[188:191], v[104:107]\n"
      "v_add_u32 v6, v10, v6\n"
      "v_mfma_f32_16x16x32_bf16 v[100:103], v[164:167], v[188:191], v[100:103]\n"
      "v_sub_u32 v10, v11, v10\n"
      "v_mfma_f32_16x16x32_bf16 v[96:99], v[168:171], v[188:191], v[96:99]\n"
      "buffer_load_dwordx4 v[156:159], v4, s[44:47], s51 offen offset:0 sc1\n"
      "s_waitcnt lgkmcnt(8)\n"
      "v_mfma_f32_16x16x32_bf16 v[104:107], v[164:167], v[192:195], v[104:107]\n"
      "v_mfma_f32_16x16x32_bf16 v[100:103], v[168:171], v[192:195], v[100:103]\n"
      "s_waitcnt lgkmcnt(7)\n"
      "v_mfma_f32_16x16x32_bf16 v[104:107], v[168:171], v[196:199], v[104:107]\n"
      "s_waitcnt vmcnt(12) lgkmcnt(6)\n"
      "v_mfma_f32_16x16x32_bf16 v[108:111], v[172:175], v[200:203], v[108:111]\n"
      "s_waitcnt lgkmcnt(5)\n"
      "v_mfma_f32_16x16x32_bf16 v[112:115], v[172:175], v[204:207], v[112:115]\n"
      "s_bitcmp1_b32 s80, s55\n"
      "s_cbranch_scc0 LPC%=\n"
      "s_waitcnt vmcnt(7)\n"
      "s_mov_b32 s71, 0x200000\n"
      "LPP%=:\n"
      "v_xor_b32 v230, v230, v228\n"
      "v_xor_b32 v231, v231, v229\n"
      "v_or_b32 v230, v230, v231\n"
      "v_cmp_ne_u32 vcc, 0, v230\n"
      "s_sub_u32 s71, s71, 1\n"
      "s_cmp_eq_u32 s71, 0\n"
      "s_cbranch_scc1 LPD%=\n"
      "s_nop 1\n"
      "s_cbranch_vccz LPD%=\n"
      "s_sleep 2\n"
      "buffer_load_dwordx2 v[230:231], v232, s[76:79], s72 offen sc1\n"
      "s_waitcnt vmcnt(0)\n"
      "s_branch LPP%=\n"
      "LPD%=:\n"
      "s_add_u32 s72, s72, 256\n"
      "LPC%=:\n"
      "s_cmp_eq_u32 s55, 2\n"
      "s_cbranch_scc1 LES%=\n"
      "LER%=:\n"
      "s_waitcnt vmcnt(9) lgkmcnt(0)\n"
      "s_barrier\n"
      "v_mfma_f32_16x16x32_bf16 v[108:111], v[176:179], v[204:207], v[108:111]\n"
      "ds_read_b128 v[188:191], v0 offset:0\n"
      "s_buffer_load_dwordx2 s[86:87], s[40:43], s81 offset:0\n"
      "v_mfma_f32_16x16x32_bf16 v[116:119], v[172:175], v[208:211], v[116:119]\n"
      "ds_read_b128 v[192:195], v0 offset:1024\n"
      "s_buffer_load_dwordx2 s[86:87], s[40:43], s81 offset:1024\n"
      "v_mfma_f32_16x16x32_bf16 v[112:115], v[176:179], v[208:211], v[112:115]\n"
      "ds_read_b128 v[196:199], v0 offset:2048\n"
      "s_add_u32 s81, s81, s82\n"
      "v_mfma_f32_16x16x32_bf16 v[108:111], v[180:183], v[208:211], v[108:111]\n"
      "ds_read_b128 v[200:203], v0 offset:3072\n"
      "s_sub_u32 s82, 128, s82\n"
      "v_mfma_f32_16x16x32_bf16 v[120:123], v[172:175], v[212:215], v[120:123]\n"
      "ds_read_b128 v[204:207], v0 offset:4096\n"
      "buffer_load_dwordx4 v[160:163], v4, s[44:47], s51 offen offset:1024 sc1\n"
      "v_mfma_f32_16x16x32_bf16 v[116:119], v[176:179], v[212:215], v[116:119]\n"
      "ds_read_b128 v[208:211], v0 offset:5120\n"
      "v_mfma_f32_16x16x32_bf16 v[112:115], v[180:183], v[212:215], v[112:115]\n"
      "buffer_load_dwordx4 v[164:167], v4, s[44:47], s51 offen offset:2048 sc1\n"
      "v_mfma_f32_16x16x32_bf16 v[108:111], v[184:187], v[212:215], v[108:111]\n"
      "v_mfma_f32_16x16x32_bf16 v[120:123], v[176:179], v[216:219], v[120:123]\n"
      "v_mfma_f32_16x16x32_bf16 v[116:119], v[180:183], v[216:219], v[116:119]\n"
      "v_mfma_f32_16x16x32_bf16 v[112:115], v[184:187], v[216:219], v[112:115]\n"
      "ds_read_b128 v[216:219], v0 offset:6144\n"
      "v_mfma_f32_16x16x32_bf16 v[120:123], v[180:183], v[220:223], v[120:123]\n"
      "s_waitcnt vmcnt(10) lgkmcnt(6)\n"
      "v_mfma_f32_16x16x32_bf16 v[116:119], v[184:187], v[220:223], v[116:119]\n"
      "ds_read_b128 v[220:223], v0 offset:7168\n"
      "buffer_load_dwordx4 v[168:171], v4, s[44:47], s51 offen offset:3072 sc1\n"
      "v_mfma_f32_16x16x32_bf16 v[120:123], v[184:187], v[224:227], v[120:123]\n"
      "ds_read_b128 v[224:227], v1 offset:0\n"
      "v_add_u32 v0, s54, v0\n"
      "s_sub_u32 s55, s55, 1\n"
      "s_cmp_lg_u32 s55, 0\n"
      "s_cbranch_scc1 LCL%=\n"
      "s_branch LFB%=\n"
      "LES%=:\n"
      "s_lshl_b32 s62, %[wv], 11\n"
      "s_add_u32 s62, s62, 57344\n"
      "v_add_u32 v228, s62, v4\n"
      "v_add_u32 v229, 65536, v228\n"
      "s_add_u32 s62, s62, 2048\n"
      "s_cmp_eq_u32 %[wv], 7\n"
      "s_cselect_b32 s62, 57360, s62\n"
      "v_add_u32 v231, s62, v4\n"
      "v_add_u32 v232, 65536, v231\n"
      "s_mov_b32 s62, %[wv]\n"
      "s_lshr_b32 s63, s62, 2\n"
      "s_mul_i32 s63, s63, 16384\n"
      "s_and_b32 s62, s62, 3\n"
      "s_lshl_b32 s62, s62, 10\n"
      "s_add_u32 s62, s62, s63\n"
      "v_add_u32 v230, s62, v4\n"
      "s_add_u32 s62, %[wv], 1\n"
      "s_and_b32 s62, s62, 7\n"
      "s_lshr_b32 s63, s62, 2\n"
      "s_mul_i32 s63, s63, 16384\n"
      "s_and_b32 s62, s62, 3\n"
      "s_lshl_b32 s62, s62, 10\n"
      "s_add_u32 s62, s62, s63\n"
      "v_add_u32 v233, s62, v4\n"
      "s_cmp_eq_u32 %[wv], 7\n"
      "s_cselect_b32 s62, 16, 0\n"
      "v_add_u32 v233, s62, v233\n"
      "s_mul_i32 s63, %[rt], 256000\n"
      "s_lshl_b32 s62, %[cb], 6\n"
      "s_add_u32 s63, s63, s62\n"
      "s_lshl_b32 s62, %[wv], 11\n"
      "s_add_u32 s63, s63, s62\n"
      "v_and_b32 v240, 0xf0, v4\n"
      "v_lshrrev_b32 v241, 8, v4\n"
      "v_lshl_add_u32 v234, v240, 10, s63\n"
      "v_lshl_add_u32 v234, v241, 4, v234\n"
      "s_lshl_b32 s62, %[wv], 2\n"
      "v_lshl_add_u32 v240, v240, 1, s62\n"
      "s_movk_i32 s62, 500\n"
      "s_cmp_eq_u32 %[rt], 15\n"
      "s_cselect_b32 s62, 485, s62\n"
      "v_cmp_gt_u32 s[94:95], s62, v240\n"
      "v_add_u32 v240, 1, v240\n"
      "v_cmp_gt_u32 s[96:97], s62, v240\n"
      "v_add_u32 v240, 1, v240\n"
      "v_cmp_gt_u32 s[98:99], s62, v240\n"
      "v_add_u32 v240, 1, v240\n"
      "v_cmp_gt_u32 s[100:101], s62, v240\n"
      "s_lshl_b32 s62, %[wv], 8\n"
      "s_add_u32 s62, s62, 155648\n"
      "v_lshrrev_b32 v240, 6, v4\n"
      "v_lshl_add_u32 v235, v240, 4, s62\n"
      "v_lshl_add_u32 v236, v241, 5, s62\n"
      "v_lshlrev_b32 v237, 5, v241\n"
      "v_add_u32 v237, 159744, v237\n"
      "v_lshrrev_b32 v238, 4, v4\n"
      "s_lshl_b32 s62, %[wv], 14\n"
      "s_add_u32 s62, s62, 57344\n"
      "v_add_u32 v242, s62, v4\n"
      "s_lshr_b32 s62, %[wv], 1\n"
      "s_lshl_b32 s62, s62, 14\n"
      "s_and_b32 s63, %[wv], 1\n"
      "s_lshl_b32 s63, s63, 10\n"
      "s_add_u32 s62, s62, s63\n"
      "s_add_u32 s62, s62, 122880\n"
      "v_add_u32 v243, s62, v4\n"
      "s_lshl_b32 s62, %[wv], 13\n"
      "v_add_u32 v244, s62, v4\n"
      "s_mov_b32 s56, 0x3ef62fad\n"
      "s_mov_b32 s57, 0xbfacc7ff\n"
      "s_mov_b32 s62, 0x3cb6d4dc\n"
      "s_mov_b32 s63, 0xc05ac6e7\n"
      "s_mov_b32 s64, 0xc0148900\n"
      "s_mov_b32 s65, 0x3b7deebf\n"
      "s_mov_b32 s66, 0xbe0b3e94\n"
      "s_mov_b32 s67, 0x3cc16524\n"
      "s_mov_b32 s68, 0x3e6623e0\n"
      "s_mov_b32 s69, 0xc0be2000\n"
      "s_mov_b32 s70, 0xbdd1af8e\n"
      "s_mov_b32 s71, 0x3e425f6a\n"
      "s_mov_b32 s72, 0x3e7e9e51\n"
      "s_mov_b32 s74, 0x3eec0625\n"
      "s_mov_b32 s76, 0x3c127a0c\n"
      "s_mov_b32 s77, 0xbbe24cb2\n"
      "s_mov_b32 s78, 0xbc500587\n"
      "s_mov_b32 s79, 0xbc20b115\n"
      "s_mov_b32 s83, 0x3f134a29\n"
      "s_mov_b32 s84, 0x4023d70a\n"
      "s_mov_b32 s85, 0x3ec80000\n"
      "s_branch LER%=\n"
      "LPROD%=:\n"
      "v_and_b32 v0, 3, v7\n"
      "v_lshrrev_b32 v1, 2, v7\n"
      "s_lshl_b32 s50, s62, 13\n"
      "s_add_u32 s50, s50, s48\n"
      "v_lshlrev_b32 v4, 15, v1\n"
      "v_lshl_add_u32 v4, v0, 4, v4\n"
      "v_add_u32 v238, s50, v4\n"
      "v_add_u32 v239, 4096, v238\n"
      "s_add_u32 s75, s49, 316\n"
      "s_min_u32 s75, s75, 8173056\n"
      "v_lshrrev_b32 v2, 2, v1\n"
      "v_and_b32 v2, 2, v2\n"
      "v_xor_b32 v2, v2, v0\n"
      "v_lshlrev_b32 v2, 4, v2\n"
      "v_lshl_add_u32 v2, v1, 6, v2\n"
      "s_lshl_b32 s51, s62, 11\n"
      "v_add_u32 v237, s51, v2\n"
      "v_add_u32 v236, 57344, v237\n"
      "s_mov_b32 s55, 4\n"
      "s_setprio 0\n"
      "v_lshlrev_b32 v240, 4, v7\n"
      "s_mov_b32 s88, 0x3f610000\n"
      "s_mov_b32 s89, 0x3f700000\n"
      "s_mov_b32 s90, 0x40a20000\n"
      "s_mov_b32 s91, 0x40100000\n"
      "s_mov_b32 s92, 0xc1735c29\n"
      "s_mov_b32 s93, 0x4200b5dd\n"
      "s_mov_b32 s83, 0xffff0000\n"
      "s_waitcnt vmcnt(0)\n"
      "buffer_load_dwordx4 v[8:11], v238, s[40:43], 0 offen offset:0\n"
      "buffer_load_dwordx4 v[12:15], v238, s[40:43], 0 offen offset:64\n"
      "buffer_load_dwordx4 v[16:19], v238, s[40:43], 0 offen offset:1024\n"
      "buffer_load_dwordx4 v[20:23], v238, s[40:43], 0 offen offset:1088\n"
      "buffer_load_dwordx4 v[24:27], v238, s[40:43], 0 offen offset:2048\n"
      "buffer_load_dwordx4 v[28:31], v238, s[40:43], 0 offen offset:2112\n"
      "buffer_load_dwordx4 v[32:35], v238, s[40:43], 0 offen offset:3072\n"
      "buffer_load_dwordx4 v[36:39], v238, s[40:43], 0 offen offset:3136\n"
      "buffer_load_dwordx4 v[40:43], v239, s[40:43], 0 offen offset:0\n"
      "buffer_load_dwordx4 v[44:47], v239, s[40:43], 0 offen offset:64\n"
      "buffer_load_dwordx4 v[48:51], v239, s[40:43], 0 offen offset:1024\n"
      "buffer_load_dwordx4 v[52:55], v239, s[40:43], 0 offen offset:1088\n"
      "buffer_load_dwordx4 v[56:59], v239, s[40:43], 0 offen offset:2048\n"
      "buffer_load_dwordx4 v[60:63], v239, s[40:43], 0 offen offset:2112\n"
      "buffer_load_dwordx4 v[64:67], v239, s[40:43], 0 offen offset:3072\n"
      "buffer_load_dwordx4 v[68:71], v239, s[40:43], 0 offen offset:3136\n"
      "s_buffer_load_dwordx2 s[86:87], s[40:43], s75 offset:0\n"
      "s_buffer_load_dwordx2 s[86:87], s[40:43], s75 offset:1024\n"
      "s_buffer_load_dwordx2 s[86:87], s[40:43], s75 offset:2048\n"
      "s_buffer_load_dwordx2 s[86:87], s[40:43], s75 offset:3072\n"
      "s_buffer_load_dwordx2 s[86:87], s[40:43], s75 offset:4096\n"
      "s_buffer_load_dwordx2 s[86:87], s[40:43], s75 offset:5120\n"
      "s_waitcnt vmcnt(8)\n"
      ".p2align 6\n"
      "LPL%=:\n"
      "s_cmp_eq_u32 s55, 4\n"
      "s_cbranch_scc1 LSE0%=\n"
      "s_buffer_load_dwordx2 s[86:87], s[40:43], s75 offset:0\n"
      "s_buffer_load_dwordx2 s[86:87], s[40:43], s75 offset:1024\n"
      "s_buffer_load_dwordx2 s[86:87], s[40:43], s75 offset:2048\n"
      "s_buffer_load_dwordx2 s[86:87], s[40:43], s75 offset:3072\n"
      "s_buffer_load_dwordx2 s[86:87], s[40:43], s75 offset:4096\n"
      "s_buffer_load_dwordx2 s[86:87], s[40:43], s75 offset:5120\n"
      "LSE0%=:\n"
      "v_lshlrev_b32 v136, 16, v8\n"
      "v_and_b32 v137, s83, v8\n"
      "v_lshlrev_b32 v138, 16, v9\n"
      "v_and_b32 v139, s83, v9\n"
      "v_lshlrev_b32 v140, 16, v10\n"
      "v_and_b32 v141, s83, v10\n"
      "v_lshlrev_b32 v142, 16, v11\n"
      "v_and_b32 v143, s83, v11\n"
      "ds_write_b128 v236, v[8:11] offset:32768\n"
      "buffer_load_dwordx4 v[72:75], v238, s[40:43], 0 offen offset:128\n"
      "v_lshlrev_b32 v152, 16, v24\n"
      "v_and_b32 v153, s83, v24\n"
      "v_lshlrev_b32 v154, 16, v25\n"
      "v_and_b32 v155, s83, v25\n"
      "v_lshlrev_b32 v156, 16, v26\n"
      "v_and_b32 v157, s83, v26\n"
      "v_lshlrev_b32 v158, 16, v27\n"
      "v_and_b32 v159, s83, v27\n"
      "v_fma_f32 v168, v152, s88, v136\n"
      "v_fma_f32 v169, v153, s88, v137\n"
      "v_fma_f32 v170, v154, s88, v138\n"
      "v_fma_f32 v171, v155, s88, v139\n"
      "v_fma_f32 v172, v156, s88, v140\n"
      "v_fma_f32 v173, v157, s88, v141\n"
      "v_fma_f32 v174, v158, s88, v142\n"
      "v_fma_f32 v175, v159, s88, v143\n"
      "v_fma_f32 v184, v152, s90, v136\n"
      "v_fma_f32 v185, v153, s90, v137\n"
      "v_fma_f32 v186, v154, s90, v138\n"
      "v_fma_f32 v187, v155, s90, v139\n"
      "v_fma_f32 v188, v156, s90, v140\n"
      "v_fma_f32 v189, v157, s90, v141\n"
      "v_fma_f32 v190, v158, s90, v142\n"
      "v_fma_f32 v191, v159, s90, v143\n"
      "v_lshlrev_b32 v160, 16, v32\n"
      "v_and_b32 v161, s83, v32\n"
      "v_lshlrev_b32 v162, 16, v33\n"
      "v_and_b32 v163, s83, v33\n"
      "v_lshlrev_b32 v164, 16, v34\n"
      "v_and_b32 v165, s83, v34\n"
      "v_lshlrev_b32 v166, 16, v35\n"
      "v_and_b32 v167, s83, v35\n"
      "ds_write_b128 v236, v[32:35] offset:40960\n"
      "buffer_load_dwordx4 v[76:79], v238, s[40:43], 0 offen offset:192\n"
      "v_lshlrev_b32 v144, 16, v16\n"
      "v_and_b32 v145, s83, v16\n"
      "v_lshlrev_b32 v146, 16, v17\n"
      "v_and_b32 v147, s83, v17\n"
      "v_lshlrev_b32 v148, 16, v18\n"
      "v_and_b32 v149, s83, v18\n"
      "v_lshlrev_b32 v150, 16, v19\n"
      "v_and_b32 v151, s83, v19\n"
      "v_fma_f32 v176, v160, s88, v144\n"
      "v_fma_f32 v177, v161, s88, v145\n"
      "v_fma_f32 v178, v162, s88, v146\n"
      "v_fma_f32 v179, v163, s88, v147\n"
      "v_fma_f32 v180, v164, s88, v148\n"
      "v_fma_f32 v181, v165, s88, v149\n"
      "v_fma_f32 v182, v166, s88, v150\n"
      "v_fma_f32 v183, v167, s88, v151\n"
      "v_fma_f32 v200, v176, s89, v168\n"
      "v_fma_f32 v201, v177, s89, v169\n"
      "v_fma_f32 v202, v178, s89, v170\n"
      "v_fma_f32 v203, v179, s89, v171\n"
      "v_fma_f32 v204, v180, s89, v172\n"
      "v_fma_f32 v205, v181, s89, v173\n"
      "v_fma_f32 v206, v182, s89, v174\n"
      "v_fma_f32 v207, v183, s89, v175\n"
      "v_cvt_pk_bf16_f32 v216, v200, v201\n"
      "v_cvt_pk_bf16_f32 v217, v202, v203\n"
      "v_cvt_pk_bf16_f32 v218, v204, v205\n"
      "v_cvt_pk_bf16_f32 v219, v206, v207\n"
      "ds_write_b128 v236, v[216:219] offset:0\n"
      "buffer_load_dwordx4 v[80:83], v238, s[40:43], 0 offen offset:1152\n"
      "v_fma_f32 v200, -v176, s89, v168\n"
      "v_fma_f32 v201, -v177, s89, v169\n"
      "v_fma_f32 v202, -v178, s89, v170\n"
      "v_fma_f32 v203, -v179, s89, v171\n"
      "v_fma_f32 v204, -v180, s89, v172\n"
      "v_fma_f32 v205, -v181, s89, v173\n"
      "v_fma_f32 v206, -v182, s89, v174\n"
      "v_fma_f32 v207, -v183, s89, v175\n"
      "v_cvt_pk_bf16_f32 v220, v200, v201\n"
      "v_cvt_pk_bf16_f32 v221, v202, v203\n"
      "v_cvt_pk_bf16_f32 v222, v204, v205\n"
      "v_cvt_pk_bf16_f32 v223, v206, v207\n"
      "ds_write_b128 v236, v[220:223] offset:8192\n"
      "v_fma_f32 v192, v160, s90, v144\n"
      "v_fma_f32 v193, v161, s90, v145\n"
      "v_fma_f32 v194, v162, s90, v146\n"
      "v_fma_f32 v195, v163, s90, v147\n"
      "v_fma_f32 v196, v164, s90, v148\n"
      "v_fma_f32 v197, v165, s90, v149\n"
      "v_fma_f32 v198, v166, s90, v150\n"
      "v_fma_f32 v199, v167, s90, v151\n"
      "v_fma_f32 v200, -v192, s91, v184\n"
      "v_fma_f32 v201, -v193, s91, v185\n"
      "v_fma_f32 v202, -v194, s91, v186\n"
      "v_fma_f32 v203, -v195, s91, v187\n"
      "v_fma_f32 v204, -v196, s91, v188\n"
      "v_fma_f32 v205, -v197, s91, v189\n"
      "v_fma_f32 v206, -v198, s91, v190\n"
      "v_fma_f32 v207, -v199, s91, v191\n"
      "v_cvt_pk_bf16_f32 v224, v200, v201\n"
      "v_cvt_pk_bf16_f32 v225, v202, v203\n"
      "v_cvt_pk_bf16_f32 v226, v204, v205\n"
      "v_cvt_pk_bf16_f32 v227, v206, v207\n"
      "ds_write_b128 v236, v[224:227] offset:24576\n"
      "buffer_load_dwordx4 v[84:87], v238, s[40:43], 0 offen offset:1216\n"
      "v_fma_f32 v208, v192, s91, v184\n"
      "v_fma_f32 v209, v193, s91, v185\n"
      "v_fma_f32 v210, v194, s91, v186\n"
      "v_fma_f32 v211, v195, s91, v187\n"
      "v_fma_f32 v212, v196, s91, v188\n"
      "v_fma_f32 v213, v197, s91, v189\n"
      "v_fma_f32 v214, v198, s91, v190\n"
      "v_fma_f32 v215, v199, s91, v191\n"
      "v_cvt_pk_bf16_f32 v228, v208, v209\n"
      "v_cvt_pk_bf16_f32 v229, v210, v211\n"
      "v_cvt_pk_bf16_f32 v230, v212, v213\n"
      "v_cvt_pk_bf16_f32 v231, v214, v215\n"
      "ds_write_b128 v236, v[228:231] offset:16384\n"
      "v_fmac_f32 v208, s92, v176\n"
      "v_fmac_f32 v209, s92, v177\n"
      "v_fmac_f32 v210, s92, v178\n"
      "v_fmac_f32 v211, s92, v179\n"
      "v_fmac_f32 v212, s92, v180\n"
      "v_fmac_f32 v213, s92, v181\n"
      "v_fmac_f32 v214, s92, v182\n"
      "v_fmac_f32 v215, s92, v183\n"
      "v_fmac_f32 v208, s93, v136\n"
      "v_fmac_f32 v209, s93, v137\n"
      "v_fmac_f32 v210, s93, v138\n"
      "v_fmac_f32 v211, s93, v139\n"
      "v_fmac_f32 v212, s93, v140\n"
      "v_fmac_f32 v213, s93, v141\n"
      "v_fmac_f32 v214, s93, v142\n"
      "v_fmac_f32 v215, s93, v143\n"
      "v_cvt_pk_bf16_f32 v232, v208, v209\n"
      "v_cvt_pk_bf16_f32 v233, v210, v211\n"
      "v_cvt_pk_bf16_f32 v234, v212, v213\n"
      "v_cvt_pk_bf16_f32 v235, v214, v215\n"
      "ds_write_b128 v236, v[232:235] offset:49152\n"
      "s_waitcnt vmcnt(4)\n"
      "v_lshlrev_b32 v136, 16, v40\n"
      "v_and_b32 v137, s83, v40\n"
      "v_lshlrev_b32 v138, 16, v41\n"
      "v_and_b32 v139, s83, v41\n"
      "v_lshlrev_b32 v140, 16, v42\n"
      "v_and_b32 v141, s83, v42\n"
      "v_lshlrev_b32 v142, 16, v43\n"
      "v_and_b32 v143, s83, v43\n"
      "ds_write_b128 v236, v[40:43] offset:33792\n"
      "buffer_load_dwordx4 v[88:91], v238, s[40:43], 0 offen offset:2176\n"
      "v_lshlrev_b32 v152, 16, v56\n"
      "v_and_b32 v153, s83, v56\n"
      "v_lshlrev_b32 v154, 16, v57\n"
      "v_and_b32 v155, s83, v57\n"
      "v_lshlrev_b32 v156, 16, v58\n"
      "v_and_b32 v157, s83, v58\n"
      "v_lshlrev_b32 v158, 16, v59\n"
      "v_and_b32 v159, s83, v59\n"
      "v_fma_f32 v168, v152, s88, v136\n"
      "v_fma_f32 v169, v153, s88, v137\n"
      "v_fma_f32 v170, v154, s88, v138\n"
      "v_fma_f32 v171, v155, s88, v139\n"
      "v_fma_f32 v172, v156, s88, v140\n"
      "v_fma_f32 v173, v157, s88, v141\n"
      "v_fma_f32 v174, v158, s88, v142\n"
      "v_fma_f32 v175, v159, s88, v143\n"
      "v_fma_f32 v184, v152, s90, v136\n"
      "v_fma_f32 v185, v153, s90, v137\n"
      "v_fma_f32 v186, v154, s90, v138\n"
      "v_fma_f32 v187, v155, s90, v139\n"
      "v_fma_f32 v188, v156, s90, v140\n"
      "v_fma_f32 v189, v157, s90, v141\n"
      "v_fma_f32 v190, v158, s90, v142\n"
      "v_fma_f32 v191, v159, s90, v143\n"
      "v_lshlrev_b32 v160, 16, v64\n"
      "v_and_b32 v161, s83, v64\n"
      "v_lshlrev_b32 v162, 16, v65\n"
      "v_and_b32 v163, s83, v65\n"
      "v_lshlrev_b32 v164, 16, v66\n"
      "v_and_b32 v165, s83, v66\n"
      "v_lshlrev_b32 v166, 16, v67\n"
      "v_and_b32 v167, s83, v67\n"
      "ds_write_b128 v236, v[64:67] offset:41984\n"
      "buffer_load_dwordx4 v[92:95], v238, s[40:43], 0 offen offset:2240\n"
      "v_lshlrev_b32 v144, 16, v48\n"
      "v_and_b32 v145, s83, v48\n"
      "v_lshlrev_b32 v146, 16, v49\n"
      "v_and_b32 v147, s83, v49\n"
      "v_lshlrev_b32 v148, 16, v50\n"
      "v_and_b32 v149, s83, v50\n"
      "v_lshlrev_b32 v150, 16, v51\n"
      "v_and_b32 v151, s83, v51\n"
      "v_fma_f32 v176, v160, s88, v144\n"
      "v_fma_f32 v177, v161, s88, v145\n"
      "v_fma_f32 v178, v162, s88, v146\n"
      "v_fma_f32 v179, v163, s88, v147\n"
      "v_fma_f32 v180, v164, s88, v148\n"
      "v_fma_f32 v181, v165, s88, v149\n"
      "v_fma_f32 v182, v166, s88, v150\n"
      "v_fma_f32 v183, v167, s88, v151\n"
      "v_fma_f32 v200, v176, s89, v168\n"
      "v_fma_f32 v201, v177, s89, v169\n"
      "v_fma_f32 v202, v178, s89, v170\n"
      "v_fma_f32 v203, v179, s89, v171\n"
      "v_fma_f32 v204, v180, s89, v172\n"
      "v_fma_f32 v205, v181, s89, v173\n"
      "v_fma_f32 v206, v182, s89, v174\n"
      "v_fma_f32 v207, v183, s89, v175\n"
      "v_cvt_pk_bf16_f32 v216, v200, v201\n"
      "v_cvt_pk_bf16_f32 v217, v202, v203\n"
      "v_cvt_pk_bf16_f32 v218, v204, v205\n"
      "v_cvt_pk_bf16_f32 v219, v206, v207\n"
      "ds_write_b128 v236, v[216:219] offset:1024\n"
      "buffer_load_dwordx4 v[96:99], v238, s[40:43], 0 offen offset:3200\n"
      "v_fma_f32 v200, -v176, s89, v168\n"
      "v_fma_f32 v201, -v177, s89, v169\n"
      "v_fma_f32 v202, -v178, s89, v170\n"
      "v_fma_f32 v203, -v179, s89, v171\n"
      "v_fma_f32 v204, -v180, s89, v172\n"
      "v_fma_f32 v205, -v181, s89, v173\n"
      "v_fma_f32 v206, -v182, s89, v174\n"
      "v_fma_f32 v207, -v183, s89, v175\n"
      "v_cvt_pk_bf16_f32 v220, v200, v201\n"
      "v_cvt_pk_bf16_f32 v221, v202, v203\n"
      "v_cvt_pk_bf16_f32 v222, v204, v205\n"
      "v_cvt_pk_bf16_f32 v223, v206, v207\n"
      "ds_write_b128 v236, v[220:223] offset:9216\n"
      "v_fma_f32 v192, v160, s90, v144\n"
      "v_fma_f32 v193, v161, s90, v145\n"
      "v_fma_f32 v194, v162, s90, v146\n"
      "v_fma_f32 v195, v163, s90, v147\n"
      "v_fma_f32 v196, v164, s90, v148\n"
      "v_fma_f32 v197, v165, s90, v149\n"
      "v_fma_f32 v198, v166, s90, v150\n"
      "v_fma_f32 v199, v167, s90, v151\n"
      "v_fma_f32 v200, -v192, s91, v184\n"
      "v_fma_f32 v201, -v193, s91, v185\n"
      "v_fma_f32 v202, -v194, s91, v186\n"
      "v_fma_f32 v203, -v195, s91, v187\n"
      "v_fma_f32 v204, -v196, s91, v188\n"
      "v_fma_f32 v205, -v197, s91, v189\n"
      "v_fma_f32 v206, -v198, s91, v190\n"
      "v_fma_f32 v207, -v199, s91, v191\n"
      "v_cvt_pk_bf16_f32 v224, v200, v201\n"
      "v_cvt_pk_bf16_f32 v225, v202, v203\n"
      "v_cvt_pk_bf16_f32 v226, v204, v205\n"
      "v_cvt_pk_bf16_f32 v227, v206, v207\n"
      "ds_write_b128 v236, v[224:227] offset:25600\n"
      "buffer_load_dwordx4 v[100:103], v238, s[40:43], 0 offen offset:3264\n"
      "v_fma_f32 v208, v192, s91, v184\n"
      "v_fma_f32 v209, v193, s91, v185\n"
      "v_fma_f32 v210, v194, s91, v186\n"
      "v_fma_f32 v211, v195, s91, v187\n"
      "v_fma_f32 v212, v196, s91, v188\n"
      "v_fma_f32 v213, v197, s91, v189\n"
      "v_fma_f32 v214, v198, s91, v190\n"
      "v_fma_f32 v215, v199, s91, v191\n"
      "v_cvt_pk_bf16_f32 v228, v208, v209\n"
      "v_cvt_pk_bf16_f32 v229, v210, v211\n"
      "v_cvt_pk_bf16_f32 v230, v212, v213\n"
      "v_cvt_pk_bf16_f32 v231, v214, v215\n"
      "ds_write_b128 v236, v[228:231] offset:17408\n"
      "v_fmac_f32 v208, s92, v176\n"
      "v_fmac_f32 v209, s92, v177\n"
      "v_fmac_f32 v210, s92, v178\n"
      "v_fmac_f32 v211, s92, v179\n"
      "v_fmac_f32 v212, s92, v180\n"
      "v_fmac_f32 v213, s92, v181\n"
      "v_fmac_f32 v214, s92, v182\n"
      "v_fmac_f32 v215, s92, v183\n"
      "v_fmac_f32 v208, s93, v136\n"
      "v_fmac_f32 v209, s93, v137\n"
      "v_fmac_f32 v210, s93, v138\n"
      "v_fmac_f32 v211, s93, v139\n"
      "v_fmac_f32 v212, s93, v140\n"
      "v_fmac_f32 v213, s93, v141\n"
      "v_fmac_f32 v214, s93, v142\n"
      "v_fmac_f32 v215, s93, v143\n"
      "v_cvt_pk_bf16_f32 v232, v208, v209\n"
      "v_cvt_pk_bf16_f32 v233, v210, v211\n"
      "v_cvt_pk_bf16_f32 v234, v212, v213\n"
      "v_cvt_pk_bf16_f32 v235, v214, v215\n"
      "ds_write_b128 v236, v[232:235] offset:50176\n"
      "s_cbranch_scc1 LNB%=\n"
      "s_waitcnt lgkmcnt(0)\n"
      "s_barrier\n"
      "LNB%=:\n"
      "s_buffer_load_dwordx2 s[86:87], s[40:43], s75 offset:6144\n"
      "s_buffer_load_dwordx2 s[86:87], s[40:43], s75 offset:7168\n"
      "s_buffer_load_dwordx2 s[86:87], s[40:43], s75 offset:8192\n"
      "s_buffer_load_dwordx2 s[86:87], s[40:43], s75 offset:9216\n"
      "s_buffer_load_dwordx2 s[86:87], s[40:43], s75 offset:10240\n"
      "s_buffer_load_dwordx2 s[86:87], s[40:43], s75 offset:11264\n"
      "v_lshlrev_b32 v136, 16, v12\n"
      "v_and_b32 v137, s83, v12\n"
      "v_lshlrev_b32 v138, 16, v13\n"
      "v_and_b32 v139, s83, v13\n"
      "v_lshlrev_b32 v140, 16, v14\n"
      "v_and_b32 v141, s83, v14\n"
      "v_lshlrev_b32 v142, 16, v15\n"
      "v_and_b32 v143, s83, v15\n"
      "ds_write_b128 v237, v[12:15] offset:32768\n"
      "buffer_load_dwordx4 v[104:107], v239, s[40:43], 0 offen offset:128\n"
      "v_lshlrev_b32 v152, 16, v28\n"
      "v_and_b32 v153, s83, v28\n"
      "v_lshlrev_b32 v154, 16, v29\n"
      "v_and_b32 v155, s83, v29\n"
      "v_lshlrev_b32 v156, 16, v30\n"
      "v_and_b32 v157, s83, v30\n"
      "v_lshlrev_b32 v158, 16, v31\n"
      "v_and_b32 v159, s83, v31\n"
      "v_fma_f32 v168, v152, s88, v136\n"
      "v_fma_f32 v169, v153, s88, v137\n"
      "v_fma_f32 v170, v154, s88, v138\n"
      "v_fma_f32 v171, v155, s88, v139\n"
      "v_fma_f32 v172, v156, s88, v140\n"
      "v_fma_f32 v173, v157, s88, v141\n"
      "v_fma_f32 v174, v158, s88, v142\n"
      "v_fma_f32 v175, v159, s88, v143\n"
      "v_fma_f32 v184, v152, s90, v136\n"
      "v_fma_f32 v185, v153, s90, v137\n"
      "v_fma_f32 v186, v154, s90, v138\n"
      "v_fma_f32 v187, v155, s90, v139\n"
      "v_fma_f32 v188, v156, s90, v140\n"
      "v_fma_f32 v189, v157, s90, v141\n"
      "v_fma_f32 v190, v158, s90, v142\n"
      "v_fma_f32 v191, v159, s90, v143\n"
      "v_lshlrev_b32 v160, 16, v36\n"
      "v_and_b32 v161, s83, v36\n"
      "v_lshlrev_b32 v162, 16, v37\n"
      "v_and_b32 v163, s83, v37\n"
      "v_lshlrev_b32 v164, 16, v38\n"
      "v_and_b32 v165, s83, v38\n"
      "v_lshlrev_b32 v166, 16, v39\n"
      "v_and_b32 v167, s83, v39\n"
      "ds_write_b128 v237, v[36:39] offset:40960\n"
      "buffer_load_dwordx4 v[108:111], v239, s[40:43], 0 offen offset:192\n"
      "v_lshlrev_b32 v144, 16, v20\n"
      "v_and_b32 v145, s83, v20\n"
      "v_lshlrev_b32 v146, 16, v21\n"
      "v_and_b32 v147, s83, v21\n"
      "v_lshlrev_b32 v148, 16, v22\n"
      "v_and_b32 v149, s83, v22\n"
      "v_lshlrev_b32 v150, 16, v23\n"
      "v_and_b32 v151, s83, v23\n"
      "v_fma_f32 v176, v160, s88, v144\n"
      "v_fma_f32 v177, v161, s88, v145\n"
      "v_fma_f32 v178, v162, s88, v146\n"
      "v_fma_f32 v179, v163, s88, v147\n"
      "v_fma_f32 v180, v164, s88, v148\n"
      "v_fma_f32 v181, v165, s88, v149\n"
      "v_fma_f32 v182, v166, s88, v150\n"
      "v_fma_f32 v183, v167, s88, v151\n"
      "v_fma_f32 v200, v176, s89, v168\n"
      "v_fma_f32 v201, v177, s89, v169\n"
      "v_fma_f32 v202, v178, s89, v170\n"
      "v_fma_f32 v203, v179, s89, v171\n"
      "v_fma_f32 v204, v180, s89, v172\n"
      "v_fma_f32 v205, v181, s89, v173\n"
      "v_fma_f32 v206, v182, s89, v174\n"
      "v_fma_f32 v207, v183, s89, v175\n"
      "v_cvt_pk_bf16_f32 v216, v200, v201\n"
      "v_cvt_pk_bf16_f32 v217, v202, v203\n"
      "v_cvt_pk_bf16_f32 v218, v204, v205\n"
      "v_cvt_pk_bf16_f32 v219, v206, v207\n"
      "ds_write_b128 v237, v[216:219] offset:0\n"
      "buffer_load_dwordx4 v[112:115], v239, s[40:43], 0 offen offset:1152\n"
      "v_fma_f32 v200, -v176, s89, v168\n"
      "v_fma_f32 v201, -v177, s89, v169\n"
      "v_fma_f32 v202, -v178, s89, v170\n"
      "v_fma_f32 v203, -v179, s89, v171\n"
      "v_fma_f32 v204, -v180, s89, v172\n"
      "v_fma_f32 v205, -v181, s89, v173\n"
      "v_fma_f32 v206, -v182, s89, v174\n"
      "v_fma_f32 v207, -v183, s89, v175\n"
      "v_cvt_pk_bf16_f32 v220, v200, v201\n"
      "v_cvt_pk_bf16_f32 v221, v202, v203\n"
      "v_cvt_pk_bf16_f32 v222, v204, v205\n"
      "v_cvt_pk_bf16_f32 v223, v206, v207\n"
      "ds_write_b128 v237, v[220:223] offset:8192\n"
      "v_fma_f32 v192, v160, s90, v144\n"
      "v_fma_f32 v193, v161, s90, v145\n"
      "v_fma_f32 v194, v162, s90, v146\n"
      "v_fma_f32 v195, v163, s90, v147\n"
      "v_fma_f32 v196, v164, s90, v148\n"
      "v_fma_f32 v197, v165, s90, v149\n"
      "v_fma_f32 v198, v166, s90, v150\n"
      "v_fma_f32 v199, v167, s90, v151\n"
      "v_fma_f32 v200, -v192, s91, v184\n"
      "v_fma_f32 v201, -v193, s91, v185\n"
      "v_fma_f32 v202, -v194, s91, v186\n"
      "v_fma_f32 v203, -v195, s91, v187\n"
      "v_fma_f32 v204, -v196, s91, v188\n"
      "v_fma_f32 v205, -v197, s91, v189\n"
      "v_fma_f32 v206, -v198, s91, v190\n"
      "v_fma_f32 v207, -v199, s91, v191\n"
      "v_cvt_pk_bf16_f32 v224, v200, v201\n"
      "v_cvt_pk_bf16_f32 v225, v202, v203\n"
      "v_cvt_pk_bf16_f32 v226, v204, v205\n"
      "v_cvt_pk_bf16_f32 v227, v206, v207\n"
      "ds_write_b128 v237, v[224:227] offset:24576\n"
      "buffer_load_dwordx4 v[116:119], v239, s[40:43], 0 offen offset:1216\n"
      "v_fma_f32 v208, v192, s91, v184\n"
      "v_fma_f32 v209, v193, s91, v185\n"
      "v_fma_f32 v210, v194, s91, v186\n"
      "v_fma_f32 v211, v195, s91, v187\n"
      "v_fma_f32 v212, v196, s91, v188\n"
      "v_fma_f32 v213, v197, s91, v189\n"
      "v_fma_f32 v214, v198, s91, v190\n"
      "v_fma_f32 v215, v199, s91, v191\n"
      "v_cvt_pk_bf16_f32 v228, v208, v209\n"
      "v_cvt_pk_bf16_f32 v229, v210, v211\n"
      "v_cvt_pk_bf16_f32 v230, v212, v213\n"
      "v_cvt_pk_bf16_f32 v231, v214, v215\n"
      "ds_write_b128 v237, v[228:231] offset:16384\n"
      "v_fmac_f32 v208, s92, v176\n"
      "v_fmac_f32 v209, s92, v177\n"
      "v_fmac_f32 v210, s92, v178\n"
      "v_fmac_f32 v211, s92, v179\n"
      "v_fmac_f32 v212, s92, v180\n"
      "v_fmac_f32 v213, s92, v181\n"
      "v_fmac_f32 v214, s92, v182\n"
      "v_fmac_f32 v215, s92, v183\n"
      "v_fmac_f32 v208, s93, v136\n"
      "v_fmac_f32 v209, s93, v137\n"
      "v_fmac_f32 v210, s93, v138\n"
      "v_fmac_f32 v211, s93, v139\n"
      "v_fmac_f32 v212, s93, v140\n"
      "v_fmac_f32 v213, s93, v141\n"
      "v_fmac_f32 v214, s93, v142\n"
      "v_fmac_f32 v215, s93, v143\n"
      "v_cvt_pk_bf16_f32 v232, v208, v209\n"
      "v_cvt_pk_bf16_f32 v233, v210, v211\n"
      "v_cvt_pk_bf16_f32 v234, v212, v213\n"
      "v_cvt_pk_bf16_f32 v235, v214, v215\n"
      "ds_write_b128 v237, v[232:235] offset:49152\n"
      "v_lshlrev_b32 v136, 16, v44\n"
      "v_and_b32 v137, s83, v44\n"
      "v_lshlrev_b32 v138, 16, v45\n"
      "v_and_b32 v139, s83, v45\n"
      "v_lshlrev_b32 v140, 16, v46\n"
      "v_and_b32 v141, s83, v46\n"
      "v_lshlrev_b32 v142, 16, v47\n"
      "v_and_b32 v143, s83, v47\n"
      "ds_write_b128 v237, v[44:47] offset:33792\n"
      "buffer_load_dwordx4 v[120:123], v239, s[40:43], 0 offen offset:2176\n"
      "v_lshlrev_b32 v152, 16, v60\n"
      "v_and_b32 v153, s83, v60\n"
      "v_lshlrev_b32 v154, 16, v61\n"
      "v_and_b32 v155, s83, v61\n"
      "v_lshlrev_b32 v156, 16, v62\n"
      "v_and_b32 v157, s83, v62\n"
      "v_lshlrev_b32 v158, 16, v63\n"
      "v_and_b32 v159, s83, v63\n"
      "v_fma_f32 v168, v152, s88, v136\n"
      "v_fma_f32 v169, v153, s88, v137\n"
      "v_fma_f32 v170, v154, s88, v138\n"
      "v_fma_f32 v171, v155, s88, v139\n"
      "v_fma_f32 v172, v156, s88, v140\n"
      "v_fma_f32 v173, v157, s88, v141\n"
      "v_fma_f32 v174, v158, s88, v142\n"
      "v_fma_f32 v175, v159, s88, v143\n"
      "v_fma_f32 v184, v152, s90, v136\n"
      "v_fma_f32 v185, v153, s90, v137\n"
      "v_fma_f32 v186, v154, s90, v138\n"
      "v_fma_f32 v187, v155, s90, v139\n"
      "v_fma_f32 v188, v156, s90, v140\n"
      "v_fma_f32 v189, v157, s90, v141\n"
      "v_fma_f32 v190, v158, s90, v142\n"
      "v_fma_f32 v191, v159, s90, v143\n"
      "v_lshlrev_b32 v160, 16, v68\n"
      "v_and_b32 v161, s83, v68\n"
      "v_lshlrev_b32 v162, 16, v69\n"
      "v_and_b32 v163, s83, v69\n"
      "v_lshlrev_b32 v164, 16, v70\n"
      "v_and_b32 v165, s83, v70\n"
      "v_lshlrev_b32 v166, 16, v71\n"
      "v_and_b32 v167, s83, v71\n"
      "ds_write_b128 v237, v[68:71] offset:41984\n"
      "buffer_load_dwordx4 v[124:127], v239, s[40:43], 0 offen offset:2240\n"
      "v_lshlrev_b32 v144, 16, v52\n"
      "v_and_b32 v145, s83, v52\n"
      "v_lshlrev_b32 v146, 16, v53\n"
      "v_and_b32 v147, s83, v53\n"
      "v_lshlrev_b32 v148, 16, v54\n"
      "v_and_b32 v149, s83, v54\n"
      "v_lshlrev_b32 v150, 16, v55\n"
      "v_and_b32 v151, s83, v55\n"
      "v_fma_f32 v176, v160, s88, v144\n"
      "v_fma_f32 v177, v161, s88, v145\n"
      "v_fma_f32 v178, v162, s88, v146\n"
      "v_fma_f32 v179, v163, s88, v147\n"
      "v_fma_f32 v180, v164, s88, v148\n"
      "v_fma_f32 v181, v165, s88, v149\n"
      "v_fma_f32 v182, v166, s88, v150\n"
      "v_fma_f32 v183, v167, s88, v151\n"
      "v_fma_f32 v200, v176, s89, v168\n"
      "v_fma_f32 v201, v177, s89, v169\n"
      "v_fma_f32 v202, v178, s89, v170\n"
      "v_fma_f32 v203, v179, s89, v171\n"
      "v_fma_f32 v204, v180, s89, v172\n"
      "v_fma_f32 v205, v181, s89, v173\n"
      "v_fma_f32 v206, v182, s89, v174\n"
      "v_fma_f32 v207, v183, s89, v175\n"
      "v_cvt_pk_bf16_f32 v216, v200, v201\n"
      "v_cvt_pk_bf16_f32 v217, v202, v203\n"
      "v_cvt_pk_bf16_f32 v218, v204, v205\n"
      "v_cvt_pk_bf16_f32 v219, v206, v207\n"
      "ds_write_b128 v237, v[216:219] offset:1024\n"
      "buffer_load_dwordx4 v[128:131], v239, s[40:43], 0 offen offset:3200\n"
      "v_fma_f32 v200, -v176, s89, v168\n"
      "v_fma_f32 v201, -v177, s89, v169\n"
      "v_fma_f32 v202, -v178, s89, v170\n"
      "v_fma_f32 v203, -v179, s89, v171\n"
      "v_fma_f32 v204, -v180, s89, v172\n"
      "v_fma_f32 v205, -v181, s89, v173\n"
      "v_fma_f32 v206, -v182, s89, v174\n"
      "v_fma_f32 v207, -v183, s89, v175\n"
      "v_cvt_pk_bf16_f32 v220, v200, v201\n"
      "v_cvt_pk_bf16_f32 v221, v202, v203\n"
      "v_cvt_pk_bf16_f32 v222, v204, v205\n"
      "v_cvt_pk_bf16_f32 v223, v206, v207\n"
      "ds_write_b128 v237, v[220:223] offset:9216\n"
      "v_fma_f32 v192, v160, s90, v144\n"
      "v_fma_f32 v193, v161, s90, v145\n"
      "v_fma_f32 v194, v162, s90, v146\n"
      "v_fma_f32 v195, v163, s90, v147\n"
      "v_fma_f32 v196, v164, s90, v148\n"
      "v_fma_f32 v197, v165, s90, v149\n"
      "v_fma_f32 v198, v166, s90, v150\n"
      "v_fma_f32 v199, v167, s90, v151\n"
      "v_fma_f32 v200, -v192, s91, v184\n"
      "v_fma_f32 v201, -v193, s91, v185\n"
      "v_fma_f32 v202, -v194, s91, v186\n"
      "v_fma_f32 v203, -v195, s91, v187\n"
      "v_fma_f32 v204, -v196, s91, v188\n"
      "v_fma_f32 v205, -v197, s91, v189\n"
      "v_fma_f32 v206, -v198, s91, v190\n"
      "v_fma_f32 v207, -v199, s91, v191\n"
      "v_cvt_pk_bf16_f32 v224, v200, v201\n"
      "v_cvt_pk_bf16_f32 v225, v202, v203\n"
      "v_cvt_pk_bf16_f32 v226, v204, v205\n"
      "v_cvt_pk_bf16_f32 v227, v206, v207\n"
      "ds_write_b128 v237, v[224:227] offset:25600\n"
      "buffer_load_dwordx4 v[132:135], v239, s[40:43], 0 offen offset:3264\n"
      "v_fma_f32 v208, v192, s91, v184\n"
      "v_fma_f32 v209, v193, s91, v185\n"
      "v_fma_f32 v210, v194, s91, v186\n"
      "v_fma_f32 v211, v195, s91, v187\n"
      "v_fma_f32 v212, v196, s91, v188\n"
      "v_fma_f32 v213, v197, s91, v189\n"
      "v_fma_f32 v214, v198, s91, v190\n"
      "v_fma_f32 v215, v199, s91, v191\n"
      "v_cvt_pk_bf16_f32 v228, v208, v209\n"
      "v_cvt_pk_bf16_f32 v229, v210, v211\n"
      "v_cvt_pk_bf16_f32 v230, v212, v213\n"
      "v_cvt_pk_bf16_f32 v231, v214, v215\n"
      "ds_write_b128 v237, v[228:231] offset:17408\n"
      "v_fmac_f32 v208, s92, v176\n"
      "v_fmac_f32 v209, s92, v177\n"
      "v_fmac_f32 v210, s92, v178\n"
      "v_fmac_f32 v211, s92, v179\n"
      "v_fmac_f32 v212, s92, v180\n"
      "v_fmac_f32 v213, s92, v181\n"
      "v_fmac_f32 v214, s92, v182\n"
      "v_fmac_f32 v215, s92, v183\n"
      "v_fmac_f32 v208, s93, v136\n"
      "v_fmac_f32 v209, s93, v137\n"
      "v_fmac_f32 v210, s93, v138\n"
      "v_fmac_f32 v211, s93, v139\n"
      "v_fmac_f32 v212, s93, v140\n"
      "v_fmac_f32 v213, s93, v141\n"
      "v_fmac_f32 v214, s93, v142\n"
      "v_fmac_f32 v215, s93, v143\n"
      "v_cvt_pk_bf16_f32 v232, v208, v209\n"
      "v_cvt_pk_bf16_f32 v233, v210, v211\n"
      "v_cvt_pk_bf16_f32 v234, v212, v213\n"
      "v_cvt_pk_bf16_f32 v235, v214, v215\n"
      "ds_write_b128 v237, v[232:235] offset:50176\n"
      "s_waitcnt vmcnt(8)\n"
      "s_waitcnt lgkmcnt(0)\n"
      "s_barrier\n"
      "s_cbranch_scc0 LNC%=\n"
      "s_barrier\n"
      "LNC%=:\n"
      "s_cmp_eq_u32 s55, 1\n"
      "s_cselect_b32 s42, 0, s42\n"
      "s_cbranch_scc0 LCW%=\n"
      "s_getpc_b64 s[84:85]\n"
      "LCX%=:\n"
      "s_add_u32 s84, s84, LPOST%=-LCX%=\n"
      "s_addc_u32 s85, s85, 0\n"
      "s_lshr_b32 s87, %[cj], 3\n"
      "s_lshl_b32 s87, s87, 2\n"
      "s_add_u32 s87, s87, %[wv]\n"
      "s_sub_u32 s87, s87, 4\n"
      "s_lshl_b32 s87, s87, 6\n"
      "s_load_dword s86, s[84:85], s87\n"
      "s_add_u32 s87, s87, 8192\n"
      "s_min_u32 s87, s87, LENDK%=-LPOST%=\n"
      "s_load_dword s86, s[84:85], s87\n"
      "LCW%=:\n"
      "v_add_u32 v238, 256, v238\n"
      "s_buffer_load_dwordx2 s[86:87], s[40:43], s75 offset:128\n"
      "s_buffer_load_dwordx2 s[86:87], s[40:43], s75 offset:1152\n"
      "s_buffer_load_dwordx2 s[86:87], s[40:43], s75 offset:2176\n"
      "s_buffer_load_dwordx2 s[86:87], s[40:43], s75 offset:3200\n"
      "s_buffer_load_dwordx2 s[86:87], s[40:43], s75 offset:4224\n"
      "s_buffer_load_dwordx2 s[86:87], s[40:43], s75 offset:5248\n"
      "v_lshlrev_b32 v136, 16, v72\n"
      "v_and_b32 v137, s83, v72\n"
      "v_lshlrev_b32 v138, 16, v73\n"
      "v_and_b32 v139, s83, v73\n"
      "v_lshlrev_b32 v140, 16, v74\n"
      "v_and_b32 v141, s83, v74\n"
      "v_lshlrev_b32 v142, 16, v75\n"
      "v_and_b32 v143, s83, v75\n"
      "ds_write_b128 v236, v[72:75] offset:32768\n"
      "buffer_load_dwordx4 v[8:11], v238, s[40:43], 0 offen offset:0\n"
      "v_lshlrev_b32 v152, 16, v88\n"
      "v_and_b32 v153, s83, v88\n"
      "v_lshlrev_b32 v154, 16, v89\n"
      "v_and_b32 v155, s83, v89\n"
      "v_lshlrev_b32 v156, 16, v90\n"
      "v_and_b32 v157, s83, v90\n"
      "v_lshlrev_b32 v158, 16, v91\n"
      "v_and_b32 v159, s83, v91\n"
      "v_fma_f32 v168, v152, s88, v136\n"
      "v_fma_f32 v169, v153, s88, v137\n"
      "v_fma_f32 v170, v154, s88, v138\n"
      "v_fma_f32 v171, v155, s88, v139\n"
      "v_fma_f32 v172, v156, s88, v140\n"
      "v_fma_f32 v173, v157, s88, v141\n"
      "v_fma_f32 v174, v158, s88, v142\n"
      "v_fma_f32 v175, v159, s88, v143\n"
      "v_fma_f32 v184, v152, s90, v136\n"
      "v_fma_f32 v185, v153, s90, v137\n"
      "v_fma_f32 v186, v154, s90, v138\n"
      "v_fma_f32 v187, v155, s90, v139\n"
      "v_fma_f32 v188, v156, s90, v140\n"
      "v_fma_f32 v189, v157, s90, v141\n"
      "v_fma_f32 v190, v158, s90, v142\n"
      "v_fma_f32 v191, v159, s90, v143\n"
      "v_lshlrev_b32 v160, 16, v96\n"
      "v_and_b32 v161, s83, v96\n"
      "v_lshlrev_b32 v162, 16, v97\n"
      "v_and_b32 v163, s83, v97\n"
      "v_lshlrev_b32 v164, 16, v98\n"
      "v_and_b32 v165, s83, v98\n"
      "v_lshlrev_b32 v166, 16, v99\n"
      "v_and_b32 v167, s83, v99\n"
      "ds_write_b128 v236, v[96:99] offset:40960\n"
      "buffer_load_dwordx4 v[12:15], v238, s[40:43], 0 offen offset:64\n"
      "v_lshlrev_b32 v144, 16, v80\n"
      "v_and_b32 v145, s83, v80\n"
      "v_lshlrev_b32 v146, 16, v81\n"
      "v_and_b32 v147, s83, v81\n"
      "v_lshlrev_b32 v148, 16, v82\n"
      "v_and_b32 v149, s83, v82\n"
      "v_lshlrev_b32 v150, 16, v83\n"
      "v_and_b32 v151, s83, v83\n"
      "v_fma_f32 v176, v160, s88, v144\n"
      "v_fma_f32 v177, v161, s88, v145\n"
      "v_fma_f32 v178, v162, s88, v146\n"
      "v_fma_f32 v179, v163, s88, v147\n"
      "v_fma_f32 v180, v164, s88, v148\n"
      "v_fma_f32 v181, v165, s88, v149\n"
      "v_fma_f32 v182, v166, s88, v150\n"
      "v_fma_f32 v183, v167, s88, v151\n"
      "v_fma_f32 v200, v176, s89, v168\n"
      "v_fma_f32 v201, v177, s89, v169\n"
      "v_fma_f32 v202, v178, s89, v170\n"
      "v_fma_f32 v203, v179, s89, v171\n"
      "v_fma_f32 v204, v180, s89, v172\n"
      "v_fma_f32 v205, v181, s89, v173\n"
      "v_fma_f32 v206, v182, s89, v174\n"
      "v_fma_f32 v207, v183, s89, v175\n"
      "v_cvt_pk_bf16_f32 v216, v200, v201\n"
      "v_cvt_pk_bf16_f32 v217, v202, v203\n"
      "v_cvt_pk_bf16_f32 v218, v204, v205\n"
      "v_cvt_pk_bf16_f32 v219, v206, v207\n"
      "ds_write_b128 v236, v[216:219] offset:0\n"
      "buffer_load_dwordx4 v[16:19], v238, s[40:43], 0 offen offset:1024\n"
      "v_fma_f32 v200, -v176, s89, v168\n"
      "v_fma_f32 v201, -v177, s89, v169\n"
      "v_fma_f32 v202, -v178, s89, v170\n"
      "v_fma_f32 v203, -v179, s89, v171\n"
      "v_fma_f32 v204, -v180, s89, v172\n"
      "v_fma_f32 v205, -v181, s89, v173\n"
      "v_fma_f32 v206, -v182, s89, v174\n"
      "v_fma_f32 v207, -v183, s89, v175\n"
      "v_cvt_pk_bf16_f32 v220, v200, v201\n"
      "v_cvt_pk_bf16_f32 v221, v202, v203\n"
      "v_cvt_pk_bf16_f32 v222, v204, v205\n"
      "v_cvt_pk_bf16_f32 v223, v206, v207\n"
      "ds_write_b128 v236, v[220:223] offset:8192\n"
      "v_fma_f32 v192, v160, s90, v144\n"
      "v_fma_f32 v193, v161, s90, v145\n"
      "v_fma_f32 v194, v162, s90, v146\n"
      "v_fma_f32 v195, v163, s90, v147\n"
      "v_fma_f32 v196, v164, s90, v148\n"
      "v_fma_f32 v197, v165, s90, v149\n"
      "v_fma_f32 v198, v166, s90, v150\n"
      "v_fma_f32 v199, v167, s90, v151\n"
      "v_fma_f32 v200, -v192, s91, v184\n"
      "v_fma_f32 v201, -v193, s91, v185\n"
      "v_fma_f32 v202, -v194, s91, v186\n"
      "v_fma_f32 v203, -v195, s91, v187\n"
      "v_fma_f32 v204, -v196, s91, v188\n"
      "v_fma_f32 v205, -v197, s91, v189\n"
      "v_fma_f32 v206, -v198, s91, v190\n"
      "v_fma_f32 v207, -v199, s91, v191\n"
      "v_cvt_pk_bf16_f32 v224, v200, v201\n"
      "v_cvt_pk_bf16_f32 v225, v202, v203\n"
      "v_cvt_pk_bf16_f32 v226, v204, v205\n"
      "v_cvt_pk_bf16_f32 v227, v206, v207\n"
      "ds_write_b128 v236, v[224:227] offset:24576\n"
      "buffer_load_dwordx4 v[20:23], v238, s[40:43], 0 offen offset:1088\n"
      "v_fma_f32 v208, v192, s91, v184\n"
      "v_fma_f32 v209, v193, s91, v185\n"
      "v_fma_f32 v210, v194, s91, v186\n"
      "v_fma_f32 v211, v195, s91, v187\n"
      "v_fma_f32 v212, v196, s91, v188\n"
      "v_fma_f32 v213, v197, s91, v189\n"
      "v_fma_f32 v214, v198, s91, v190\n"
      "v_fma_f32 v215, v199, s91, v191\n"
      "v_cvt_pk_bf16_f32 v228, v208, v209\n"
      "v_cvt_pk_bf16_f32 v229, v210, v211\n"
      "v_cvt_pk_bf16_f32 v230, v212, v213\n"
      "v_cvt_pk_bf16_f32 v231, v214, v215\n"
      "ds_write_b128 v236, v[228:231] offset:16384\n"
      "v_fmac_f32 v208, s92, v176\n"
      "v_fmac_f32 v209, s92, v177\n"
      "v_fmac_f32 v210, s92, v178\n"
      "v_fmac_f32 v211, s92, v179\n"
      "v_fmac_f32 v212, s92, v180\n"
      "v_fmac_f32 v213, s92, v181\n"
      "v_fmac_f32 v214, s92, v182\n"
      "v_fmac_f32 v215, s92, v183\n"
      "v_fmac_f32 v208, s93, v136\n"
      "v_fmac_f32 v209, s93, v137\n"
      "v_fmac_f32 v210, s93, v138\n"
      "v_fmac_f32 v211, s93, v139\n"
      "v_fmac_f32 v212, s93, v140\n"
      "v_fmac_f32 v213, s93, v141\n"
      "v_fmac_f32 v214, s93, v142\n"
      "v_fmac_f32 v215, s93, v143\n"
      "v_cvt_pk_bf16_f32 v232, v208, v209\n"
      "v_cvt_pk_bf16_f32 v233, v210, v211\n"
      "v_cvt_pk_bf16_f32 v234, v212, v213\n"
      "v_cvt_pk_bf16_f32 v235, v214, v215\n"
      "ds_write_b128 v236, v[232:235] offset:49152\n"
      "s_waitcnt vmcnt(4)\n"
      "v_lshlrev_b32 v136, 16, v104\n"
      "v_and_b32 v137, s83, v104\n"
      "v_lshlrev_b32 v138, 16, v105\n"
      "v_and_b32 v139, s83, v105\n"
      "v_lshlrev_b32 v140, 16, v106\n"
      "v_and_b32 v141, s83, v106\n"
      "v_lshlrev_b32 v142, 16, v107\n"
      "v_and_b32 v143, s83, v107\n"
      "ds_write_b128 v236, v[104:107] offset:33792\n"
      "buffer_load_dwordx4 v[24:27], v238, s[40:43], 0 offen offset:2048\n"
      "v_lshlrev_b32 v152, 16, v120\n"
      "v_and_b32 v153, s83, v120\n"
      "v_lshlrev_b32 v154, 16, v121\n"
      "v_and_b32 v155, s83, v121\n"
      "v_lshlrev_b32 v156, 16, v122\n"
      "v_and_b32 v157, s83, v122\n"
      "v_lshlrev_b32 v158, 16, v123\n"
      "v_and_b32 v159, s83, v123\n"
      "v_fma_f32 v168, v152, s88, v136\n"
      "v_fma_f32 v169, v153, s88, v137\n"
      "v_fma_f32 v170, v154, s88, v138\n"
      "v_fma_f32 v171, v155, s88, v139\n"
      "v_fma_f32 v172, v156, s88, v140\n"
      "v_fma_f32 v173, v157, s88, v141\n"
      "v_fma_f32 v174, v158, s88, v142\n"
      "v_fma_f32 v175, v159, s88, v143\n"
      "v_fma_f32 v184, v152, s90, v136\n"
      "v_fma_f32 v185, v153, s90, v137\n"
      "v_fma_f32 v186, v154, s90, v138\n"
      "v_fma_f32 v187, v155, s90, v139\n"
      "v_fma_f32 v188, v156, s90, v140\n"
      "v_fma_f32 v189, v157, s90, v141\n"
      "v_fma_f32 v190, v158, s90, v142\n"
      "v_fma_f32 v191, v159, s90, v143\n"
      "v_lshlrev_b32 v160, 16, v128\n"
      "v_and_b32 v161, s83, v128\n"
      "v_lshlrev_b32 v162, 16, v129\n"
      "v_and_b32 v163, s83, v129\n"
      "v_lshlrev_b32 v164, 16, v130\n"
      "v_and_b32 v165, s83, v130\n"
      "v_lshlrev_b32 v166, 16, v131\n"
      "v_and_b32 v167, s83, v131\n"
      "ds_write_b128 v236, v[128:131] offset:41984\n"
      "buffer_load_dwordx4 v[28:31], v238, s[40:43], 0 offen offset:2112\n"
      "v_lshlrev_b32 v144, 16, v112\n"
      "v_and_b32 v145, s83, v112\n"
      "v_lshlrev_b32 v146, 16, v113\n"
      "v_and_b32 v147, s83, v113\n"
      "v_lshlrev_b32 v148, 16, v114\n"
      "v_and_b32 v149, s83, v114\n"
      "v_lshlrev_b32 v150, 16, v115\n"
      "v_and_b32 v151, s83, v115\n"
      "v_fma_f32 v176, v160, s88, v144\n"
      "v_fma_f32 v177, v161, s88, v145\n"
      "v_fma_f32 v178, v162, s88, v146\n"
      "v_fma_f32 v179, v163, s88, v147\n"
      "v_fma_f32 v180, v164, s88, v148\n"
      "v_fma_f32 v181, v165, s88, v149\n"
      "v_fma_f32 v182, v166, s88, v150\n"
      "v_fma_f32 v183, v167, s88, v151\n"
      "v_fma_f32 v200, v176, s89, v168\n"
      "v_fma_f32 v201, v177, s89, v169\n"
      "v_fma_f32 v202, v178, s89, v170\n"
      "v_fma_f32 v203, v179, s89, v171\n"
      "v_fma_f32 v204, v180, s89, v172\n"
      "v_fma_f32 v205, v181, s89, v173\n"
      "v_fma_f32 v206, v182, s89, v174\n"
      "v_fma_f32 v207, v183, s89, v175\n"
      "v_cvt_pk_bf16_f32 v216, v200, v201\n"
      "v_cvt_pk_bf16_f32 v217, v202, v203\n"
      "v_cvt_pk_bf16_f32 v218, v204, v205\n"
      "v_cvt_pk_bf16_f32 v219, v206, v207\n"
      "ds_write_b128 v236, v[216:219] offset:1024\n"
      "buffer_load_dwordx4 v[32:35], v238, s[40:43], 0 offen offset:3072\n"
      "v_fma_f32 v200, -v176, s89, v168\n"
      "v_fma_f32 v201, -v177, s89, v169\n"
      "v_fma_f32 v202, -v178, s89, v170\n"
      "v_fma_f32 v203, -v179, s89, v171\n"
      "v_fma_f32 v204, -v180, s89, v172\n"
      "v_fma_f32 v205, -v181, s89, v173\n"
      "v_fma_f32 v206, -v182, s89, v174\n"
      "v_fma_f32 v207, -v183, s89, v175\n"
      "v_cvt_pk_bf16_f32 v220, v200, v201\n"
      "v_cvt_pk_bf16_f32 v221, v202, v203\n"
      "v_cvt_pk_bf16_f32 v222, v204, v205\n"
      "v_cvt_pk_bf16_f32 v223, v206, v207\n"
      "ds_write_b128 v236, v[220:223] offset:9216\n"
      "v_fma_f32 v192, v160, s90, v144\n"
      "v_fma_f32 v193, v161, s90, v145\n"
      "v_fma_f32 v194, v162, s90, v146\n"
      "v_fma_f32 v195, v163, s90, v147\n"
      "v_fma_f32 v196, v164, s90, v148\n"
      "v_fma_f32 v197, v165, s90, v149\n"
      "v_fma_f32 v198, v166, s90, v150\n"
      "v_fma_f32 v199, v167, s90, v151\n"
      "v_fma_f32 v200, -v192, s91, v184\n"
      "v_fma_f32 v201, -v193, s91, v185\n"
      "v_fma_f32 v202, -v194, s91, v186\n"
      "v_fma_f32 v203, -v195, s91, v187\n"
      "v_fma_f32 v204, -v196, s91, v188\n"
      "v_fma_f32 v205, -v197, s91, v189\n"
      "v_fma_f32 v206, -v198, s91, v190\n"
      "v_fma_f32 v207, -v199, s91, v191\n"
      "v_cvt_pk_bf16_f32 v224, v200, v201\n"
      "v_cvt_pk_bf16_f32 v225, v202, v203\n"
      "v_cvt_pk_bf16_f32 v226, v204, v205\n"
      "v_cvt_pk_bf16_f32 v227, v206, v207\n"
      "ds_write_b128 v236, v[224:227] offset:25600\n"
      "buffer_load_dwordx4 v[36:39], v238, s[40:43], 0 offen offset:3136\n"
      "v_fma_f32 v208, v192, s91, v184\n"
      "v_fma_f32 v209, v193, s91, v185\n"
      "v_fma_f32 v210, v194, s91, v186\n"
      "v_fma_f32 v211, v195, s91, v187\n"
      "v_fma_f32 v212, v196, s91, v188\n"
      "v_fma_f32 v213, v197, s91, v189\n"
      "v_fma_f32 v214, v198, s91, v190\n"
      "v_fma_f32 v215, v199, s91, v191\n"
      "v_cvt_pk_bf16_f32 v228, v208, v209\n"
      "v_cvt_pk_bf16_f32 v229, v210, v211\n"
      "v_cvt_pk_bf16_f32 v230, v212, v213\n"
      "v_cvt_pk_bf16_f32 v231, v214, v215\n"
      "ds_write_b128 v236, v[228:231] offset:17408\n"
      "v_fmac_f32 v208, s92, v176\n"
      "v_fmac_f32 v209, s92, v177\n"
      "v_fmac_f32 v210, s92, v178\n"
      "v_fmac_f32 v211, s92, v179\n"
      "v_fmac_f32 v212, s92, v180\n"
      "v_fmac_f32 v213, s92, v181\n"
      "v_fmac_f32 v214, s92, v182\n"
      "v_fmac_f32 v215, s92, v183\n"
      "v_fmac_f32 v208, s93, v136\n"
      "v_fmac_f32 v209, s93, v137\n"
      "v_fmac_f32 v210, s93, v138\n"
      "v_fmac_f32 v211, s93, v139\n"
      "v_fmac_f32 v212, s93, v140\n"
      "v_fmac_f32 v213, s93, v141\n"
      "v_fmac_f32 v214, s93, v142\n"
      "v_fmac_f32 v215, s93, v143\n"
      "v_cvt_pk_bf16_f32 v232, v208, v209\n"
      "v_cvt_pk_bf16_f32 v233, v210, v211\n"
      "v_cvt_pk_bf16_f32 v234, v212, v213\n"
      "v_cvt_pk_bf16_f32 v235, v214, v215\n"
      "ds_write_b128 v236, v[232:235] offset:50176\n"
      "s_waitcnt lgkmcnt(0)\n"
      "s_barrier\n"
      "v_add_u32 v239, 256, v239\n"
      "s_buffer_load_dwordx2 s[86:87], s[40:43], s75 offset:6272\n"
      "s_buffer_load_dwordx2 s[86:87], s[40:43], s75 offset:7296\n"
      "s_buffer_load_dwordx2 s[86:87], s[40:43], s75 offset:8320\n"
      "s_buffer_load_dwordx2 s[86:87], s[40:43], s75 offset:9344\n"
      "s_buffer_load_dwordx2 s[86:87], s[40:43], s75 offset:10368\n"
      "s_buffer_load_dwordx2 s[86:87], s[40:43], s75 offset:11392\n"
      "v_lshlrev_b32 v136, 16, v76\n"
      "v_and_b32 v137, s83, v76\n"
      "v_lshlrev_b32 v138, 16, v77\n"
      "v_and_b32 v139, s83, v77\n"
      "v_lshlrev_b32 v140, 16, v78\n"
      "v_and_b32 v141, s83, v78\n"
      "v_lshlrev_b32 v142, 16, v79\n"
      "v_and_b32 v143, s83, v79\n"
      "ds_write_b128 v237, v[76:79] offset:32768\n"
      "buffer_load_dwordx4 v[40:43], v239, s[40:43], 0 offen offset:0\n"
      "v_lshlrev_b32 v152, 16, v92\n"
      "v_and_b32 v153, s83, v92\n"
      "v_lshlrev_b32 v154, 16, v93\n"
      "v_and_b32 v155, s83, v93\n"
      "v_lshlrev_b32 v156, 16, v94\n"
      "v_and_b32 v157, s83, v94\n"
      "v_lshlrev_b32 v158, 16, v95\n"
      "v_and_b32 v159, s83, v95\n"
      "v_fma_f32 v168, v152, s88, v136\n"
      "v_fma_f32 v169, v153, s88, v137\n"
      "v_fma_f32 v170, v154, s88, v138\n"
      "v_fma_f32 v171, v155, s88, v139\n"
      "v_fma_f32 v172, v156, s88, v140\n"
      "v_fma_f32 v173, v157, s88, v141\n"
      "v_fma_f32 v174, v158, s88, v142\n"
      "v_fma_f32 v175, v159, s88, v143\n"
      "v_fma_f32 v184, v152, s90, v136\n"
      "v_fma_f32 v185, v153, s90, v137\n"
      "v_fma_f32 v186, v154, s90, v138\n"
      "v_fma_f32 v187, v155, s90, v139\n"
      "v_fma_f32 v188, v156, s90, v140\n"
      "v_fma_f32 v189, v157, s90, v141\n"
      "v_fma_f32 v190, v158, s90, v142\n"
      "v_fma_f32 v191, v159, s90, v143\n"
      "v_lshlrev_b32 v160, 16, v100\n"
      "v_and_b32 v161, s83, v100\n"
      "v_lshlrev_b32 v162, 16, v101\n"
      "v_and_b32 v163, s83, v101\n"
      "v_lshlrev_b32 v164, 16, v102\n"
      "v_and_b32 v165, s83, v102\n"
      "v_lshlrev_b32 v166, 16, v103\n"
      "v_and_b32 v167, s83, v103\n"
      "ds_write_b128 v237, v[100:103] offset:40960\n"
      "buffer_load_dwordx4 v[44:47], v239, s[40:43], 0 offen offset:64\n"
      "v_lshlrev_b32 v144, 16, v84\n"
      "v_and_b32 v145, s83, v84\n"
      "v_lshlrev_b32 v146, 16, v85\n"
      "v_and_b32 v147, s83, v85\n"
      "v_lshlrev_b32 v148, 16, v86\n"
      "v_and_b32 v149, s83, v86\n"
      "v_lshlrev_b32 v150, 16, v87\n"
      "v_and_b32 v151, s83, v87\n"
      "v_fma_f32 v176, v160, s88, v144\n"
      "v_fma_f32 v177, v161, s88, v145\n"
      "v_fma_f32 v178, v162, s88, v146\n"
      "v_fma_f32 v179, v163, s88, v147\n"
      "v_fma_f32 v180, v164, s88, v148\n"
      "v_fma_f32 v181, v165, s88, v149\n"
      "v_fma_f32 v182, v166, s88, v150\n"
      "v_fma_f32 v183, v167, s88, v151\n"
      "v_fma_f32 v200, v176, s89, v168\n"
      "v_fma_f32 v201, v177, s89, v169\n"
      "v_fma_f32 v202, v178, s89, v170\n"
      "v_fma_f32 v203, v179, s89, v171\n"
      "v_fma_f32 v204, v180, s89, v172\n"
      "v_fma_f32 v205, v181, s89, v173\n"
      "v_fma_f32 v206, v182, s89, v174\n"
      "v_fma_f32 v207, v183, s89, v175\n"
      "v_cvt_pk_bf16_f32 v216, v200, v201\n"
      "v_cvt_pk_bf16_f32 v217, v202, v203\n"
      "v_cvt_pk_bf16_f32 v218, v204, v205\n"
      "v_cvt_pk_bf16_f32 v219, v206, v207\n"
      "ds_write_b128 v237, v[216:219] offset:0\n"
      "buffer_load_dwordx4 v[48:51], v239, s[40:43], 0 offen offset:1024\n"
      "v_fma_f32 v200, -v176, s89, v168\n"
      "v_fma_f32 v201, -v177, s89, v169\n"
      "v_fma_f32 v202, -v178, s89, v170\n"
      "v_fma_f32 v203, -v179, s89, v171\n"
      "v_fma_f32 v204, -v180, s89, v172\n"
      "v_fma_f32 v205, -v181, s89, v173\n"
      "v_fma_f32 v206, -v182, s89, v174\n"
      "v_fma_f32 v207, -v183, s89, v175\n"
      "v_cvt_pk_bf16_f32 v220, v200, v201\n"
      "v_cvt_pk_bf16_f32 v221, v202, v203\n"
      "v_cvt_pk_bf16_f32 v222, v204, v205\n"
      "v_cvt_pk_bf16_f32 v223, v206, v207\n"
      "ds_write_b128 v237, v[220:223] offset:8192\n"
      "v_fma_f32 v192, v160, s90, v144\n"
      "v_fma_f32 v193, v161, s90, v145\n"
      "v_fma_f32 v194, v162, s90, v146\n"
      "v_fma_f32 v195, v163, s90, v147\n"
      "v_fma_f32 v196, v164, s90, v148\n"
      "v_fma_f32 v197, v165, s90, v149\n"
      "v_fma_f32 v198, v166, s90, v150\n"
      "v_fma_f32 v199, v167, s90, v151\n"
      "v_fma_f32 v200, -v192, s91, v184\n"
      "v_fma_f32 v201, -v193, s91, v185\n"
      "v_fma_f32 v202, -v194, s91, v186\n"
      "v_fma_f32 v203, -v195, s91, v187\n"
      "v_fma_f32 v204, -v196, s91, v188\n"
      "v_fma_f32 v205, -v197, s91, v189\n"
      "v_fma_f32 v206, -v198, s91, v190\n"
      "v_fma_f32 v207, -v199, s91, v191\n"
      "v_cvt_pk_bf16_f32 v224, v200, v201\n"
      "v_cvt_pk_bf16_f32 v225, v202, v203\n"
      "v_cvt_pk_bf16_f32 v226, v204, v205\n"
      "v_cvt_pk_bf16_f32 v227, v206, v207\n"
      "ds_write_b128 v237, v[224:227] offset:24576\n"
      "buffer_load_dwordx4 v[52:55], v239, s[40:43], 0 offen offset:1088\n"
      "v_fma_f32 v208, v192, s91, v184\n"
      "v_fma_f32 v209, v193, s91, v185\n"
      "v_fma_f32 v210, v194, s91, v186\n"
      "v_fma_f32 v211, v195, s91, v187\n"
      "v_fma_f32 v212, v196, s91, v188\n"
      "v_fma_f32 v213, v197, s91, v189\n"
      "v_fma_f32 v214, v198, s91, v190\n"
      "v_fma_f32 v215, v199, s91, v191\n"
      "v_cvt_pk_bf16_f32 v228, v208, v209\n"
      "v_cvt_pk_bf16_f32 v229, v210, v211\n"
      "v_cvt_pk_bf16_f32 v230, v212, v213\n"
      "v_cvt_pk_bf16_f32 v231, v214, v215\n"
      "ds_write_b128 v237, v[228:231] offset:16384\n"
      "v_fmac_f32 v208, s92, v176\n"
      "v_fmac_f32 v209, s92, v177\n"
      "v_fmac_f32 v210, s92, v178\n"
      "v_fmac_f32 v211, s92, v179\n"
      "v_fmac_f32 v212, s92, v180\n"
      "v_fmac_f32 v213, s92, v181\n"
      "v_fmac_f32 v214, s92, v182\n"
      "v_fmac_f32 v215, s92, v183\n"
      "v_fmac_f32 v208, s93, v136\n"
      "v_fmac_f32 v209, s93, v137\n"
      "v_fmac_f32 v210, s93, v138\n"
      "v_fmac_f32 v211, s93, v139\n"
      "v_fmac_f32 v212, s93, v140\n"
      "v_fmac_f32 v213, s93, v141\n"
      "v_fmac_f32 v214, s93, v142\n"
      "v_fmac_f32 v215, s93, v143\n"
      "v_cvt_pk_bf16_f32 v232, v208, v209\n"
      "v_cvt_pk_bf16_f32 v233, v210, v211\n"
      "v_cvt_pk_bf16_f32 v234, v212, v213\n"
      "v_cvt_pk_bf16_f32 v235, v214, v215\n"
      "ds_write_b128 v237, v[232:235] offset:49152\n"
      "v_lshlrev_b32 v136, 16, v108\n"
      "v_and_b32 v137, s83, v108\n"
      "v_lshlrev_b32 v138, 16, v109\n"
      "v_and_b32 v139, s83, v109\n"
      "v_lshlrev_b32 v140, 16, v110\n"
      "v_and_b32 v141, s83, v110\n"
      "v_lshlrev_b32 v142, 16, v111\n"
      "v_and_b32 v143, s83, v111\n"
      "ds_write_b128 v237, v[108:111] offset:33792\n"
      "buffer_load_dwordx4 v[56:59], v239, s[40:43], 0 offen offset:2048\n"
      "v_lshlrev_b32 v152, 16, v124\n"
      "v_and_b32 v153, s83, v124\n"
      "v_lshlrev_b32 v154, 16, v125\n"
      "v_and_b32 v155, s83, v125\n"
      "v_lshlrev_b32 v156, 16, v126\n"
      "v_and_b32 v157, s83, v126\n"
      "v_lshlrev_b32 v158, 16, v127\n"
      "v_and_b32 v159, s83, v127\n"
      "v_fma_f32 v168, v152, s88, v136\n"
      "v_fma_f32 v169, v153, s88, v137\n"
      "v_fma_f32 v170, v154, s88, v138\n"
      "v_fma_f32 v171, v155, s88, v139\n"
      "v_fma_f32 v172, v156, s88, v140\n"
      "v_fma_f32 v173, v157, s88, v141\n"
      "v_fma_f32 v174, v158, s88, v142\n"
      "v_fma_f32 v175, v159, s88, v143\n"
      "v_fma_f32 v184, v152, s90, v136\n"
      "v_fma_f32 v185, v153, s90, v137\n"
      "v_fma_f32 v186, v154, s90, v138\n"
      "v_fma_f32 v187, v155, s90, v139\n"
      "v_fma_f32 v188, v156, s90, v140\n"
      "v_fma_f32 v189, v157, s90, v141\n"
      "v_fma_f32 v190, v158, s90, v142\n"
      "v_fma_f32 v191, v159, s90, v143\n"
      "v_lshlrev_b32 v160, 16, v132\n"
      "v_and_b32 v161, s83, v132\n"
      "v_lshlrev_b32 v162, 16, v133\n"
      "v_and_b32 v163, s83, v133\n"
      "v_lshlrev_b32 v164, 16, v134\n"
      "v_and_b32 v165, s83, v134\n"
      "v_lshlrev_b32 v166, 16, v135\n"
      "v_and_b32 v167, s83, v135\n"
      "ds_write_b128 v237, v[132:135] offset:41984\n"
      "buffer_load_dwordx4 v[60:63], v239, s[40:43], 0 offen offset:2112\n"
      "v_lshlrev_b32 v144, 16, v116\n"
      "v_and_b32 v145, s83, v116\n"
      "v_lshlrev_b32 v146, 16, v117\n"
      "v_and_b32 v147, s83, v117\n"
      "v_lshlrev_b32 v148, 16, v118\n"
      "v_and_b32 v149, s83, v118\n"
      "v_lshlrev_b32 v150, 16, v119\n"
      "v_and_b32 v151, s83, v119\n"
      "v_fma_f32 v176, v160, s88, v144\n"
      "v_fma_f32 v177, v161, s88, v145\n"
      "v_fma_f32 v178, v162, s88, v146\n"
      "v_fma_f32 v179, v163, s88, v147\n"
      "v_fma_f32 v180, v164, s88, v148\n"
      "v_fma_f32 v181, v165, s88, v149\n"
      "v_fma_f32 v182, v166, s88, v150\n"
      "v_fma_f32 v183, v167, s88, v151\n"
      "v_fma_f32 v200, v176, s89, v168\n"
      "v_fma_f32 v201, v177, s89, v169\n"
      "v_fma_f32 v202, v178, s89, v170\n"
      "v_fma_f32 v203, v179, s89, v171\n"
      "v_fma_f32 v204, v180, s89, v172\n"
      "v_fma_f32 v205, v181, s89, v173\n"
      "v_fma_f32 v206, v182, s89, v174\n"
      "v_fma_f32 v207, v183, s89, v175\n"
      "v_cvt_pk_bf16_f32 v216, v200, v201\n"
      "v_cvt_pk_bf16_f32 v217, v202, v203\n"
      "v_cvt_pk_bf16_f32 v218, v204, v205\n"
      "v_cvt_pk_bf16_f32 v219, v206, v207\n"
      "ds_write_b128 v237, v[216:219] offset:1024\n"
      "buffer_load_dwordx4 v[64:67], v239, s[40:43], 0 offen offset:3072\n"
      "v_fma_f32 v200, -v176, s89, v168\n"
      "v_fma_f32 v201, -v177, s89, v169\n"
      "v_fma_f32 v202, -v178, s89, v170\n"
      "v_fma_f32 v203, -v179, s89, v171\n"
      "v_fma_f32 v204, -v180, s89, v172\n"
      "v_fma_f32 v205, -v181, s89, v173\n"
      "v_fma_f32 v206, -v182, s89, v174\n"
      "v_fma_f32 v207, -v183, s89, v175\n"
      "v_cvt_pk_bf16_f32 v220, v200, v201\n"
      "v_cvt_pk_bf16_f32 v221, v202, v203\n"
      "v_cvt_pk_bf16_f32 v222, v204, v205\n"
      "v_cvt_pk_bf16_f32 v223, v206, v207\n"
      "ds_write_b128 v237, v[220:223] offset:9216\n"
      "v_fma_f32 v192, v160, s90, v144\n"
      "v_fma_f32 v193, v161, s90, v145\n"
      "v_fma_f32 v194, v162, s90, v146\n"
      "v_fma_f32 v195, v163, s90, v147\n"
      "v_fma_f32 v196, v164, s90, v148\n"
      "v_fma_f32 v197, v165, s90, v149\n"
      "v_fma_f32 v198, v166, s90, v150\n"
      "v_fma_f32 v199, v167, s90, v151\n"
      "v_fma_f32 v200, -v192, s91, v184\n"
      "v_fma_f32 v201, -v193, s91, v185\n"
      "v_fma_f32 v202, -v194, s91, v186\n"
      "v_fma_f32 v203, -v195, s91, v187\n"
      "v_fma_f32 v204, -v196, s91, v188\n"
      "v_fma_f32 v205, -v197, s91, v189\n"
      "v_fma_f32 v206, -v198, s91, v190\n"
      "v_fma_f32 v207, -v199, s91, v191\n"
      "v_cvt_pk_bf16_f32 v224, v200, v201\n"
      "v_cvt_pk_bf16_f32 v225, v202, v203\n"
      "v_cvt_pk_bf16_f32 v226, v204, v205\n"
      "v_cvt_pk_bf16_f32 v227, v206, v207\n"
      "ds_write_b128 v237, v[224:227] offset:25600\n"
      "buffer_load_dwordx4 v[68:71], v239, s[40:43], 0 offen offset:3136\n"
      "v_fma_f32 v208, v192, s91, v184\n"
      "v_fma_f32 v209, v193, s91, v185\n"
      "v_fma_f32 v210, v194, s91, v186\n"
      "v_fma_f32 v211, v195, s91, v187\n"
      "v_fma_f32 v212, v196, s91, v188\n"
      "v_fma_f32 v213, v197, s91, v189\n"
      "v_fma_f32 v214, v198, s91, v190\n"
      "v_fma_f32 v215, v199, s91, v191\n"
      "v_cvt_pk_bf16_f32 v228, v208, v209\n"
      "v_cvt_pk_bf16_f32 v229, v210, v211\n"
      "v_cvt_pk_bf16_f32 v230, v212, v213\n"
      "v_cvt_pk_bf16_f32 v231, v214, v215\n"
      "ds_write_b128 v237, v[228:231] offset:17408\n"
      "v_fmac_f32 v208, s92, v176\n"
      "v_fmac_f32 v209, s92, v177\n"
      "v_fmac_f32 v210, s92, v178\n"
      "v_fmac_f32 v211, s92, v179\n"
      "v_fmac_f32 v212, s92, v180\n"
      "v_fmac_f32 v213, s92, v181\n"
      "v_fmac_f32 v214, s92, v182\n"
      "v_fmac_f32 v215, s92, v183\n"
      "v_fmac_f32 v208, s93, v136\n"
      "v_fmac_f32 v209, s93, v137\n"
      "v_fmac_f32 v210, s93, v138\n"
      "v_fmac_f32 v211, s93, v139\n"
      "v_fmac_f32 v212, s93, v140\n"
      "v_fmac_f32 v213, s93, v141\n"
      "v_fmac_f32 v214, s93, v142\n"
      "v_fmac_f32 v215, s93, v143\n"
      "v_cvt_pk_bf16_f32 v232, v208, v209\n"
      "v_cvt_pk_bf16_f32 v233, v210, v211\n"
      "v_cvt_pk_bf16_f32 v234, v212, v213\n"
      "v_cvt_pk_bf16_f32 v235, v214, v215\n"
      "ds_write_b128 v237, v[232:235] offset:50176\n"
      "s_waitcnt vmcnt(8)\n"
      "s_waitcnt lgkmcnt(0)\n"
      "s_barrier\n"
      "s_add_u32 s75, s75, 256\n"
      "s_sub_u32 s55, s55, 1\n"
      "s_cmp_lg_u32 s55, 0\n"
      "s_cbranch_scc1 LPL%=\n"
      "LPOST%=:\n"
      "s_waitcnt vmcnt(0)\n"
      "s_lshl_b32 s62, %[wv], 11\n"
      "s_add_u32 s62, s62, 57344\n"
      "v_add_u32 v228, s62, v240\n"
      "v_add_u32 v229, 65536, v228\n"
      "s_add_u32 s62, s62, 2048\n"
      "s_cmp_eq_u32 %[wv], 7\n"
      "s_cselect_b32 s62, 57360, s62\n"
      "v_add_u32 v231, s62, v240\n"
      "v_add_u32 v232, 65536, v231\n"
      "s_mov_b32 s62, %[wv]\n"
      "s_lshr_b32 s63, s62, 2\n"
      "s_mul_i32 s63, s63, 16384\n"
      "s_and_b32 s62, s62, 3\n"
      "s_lshl_b32 s62, s62, 10\n"
      "s_add_u32 s62, s62, s63\n"
      "v_add_u32 v230, s62, v240\n"
      "s_add_u32 s62, %[wv], 1\n"
      "s_and_b32 s62, s62, 7\n"
      "s_lshr_b32 s63, s62, 2\n"
      "s_mul_i32 s63, s63, 16384\n"
      "s_and_b32 s62, s62, 3\n"
      "s_lshl_b32 s62, s62, 10\n"
      "s_add_u32 s62, s62, s63\n"
      "v_add_u32 v233, s62, v240\n"
      "s_cmp_eq_u32 %[wv], 7\n"
      "s_cselect_b32 s62, 16, 0\n"
      "v_add_u32 v233, s62, v233\n"
      "s_mul_i32 s63, %[rt], 256000\n"
      "s_lshl_b32 s62, %[cb], 6\n"
      "s_add_u32 s63, s63, s62\n"
      "s_lshl_b32 s62, %[wv], 11\n"
      "s_add_u32 s63, s63, s62\n"
      "v_and_b32 v8, 0xf0, v240\n"
      "v_lshrrev_b32 v9, 8, v240\n"
      "v_lshl_add_u32 v234, v8, 10, s63\n"
      "v_lshl_add_u32 v234, v9, 4, v234\n"
      "s_lshl_b32 s62, %[wv], 2\n"
      "v_lshl_add_u32 v8, v8, 1, s62\n"
      "s_movk_i32 s62, 500\n"
      "s_cmp_eq_u32 %[rt], 15\n"
      "s_cselect_b32 s62, 485, s62\n"
      "v_cmp_gt_u32 s[94:95], s62, v8\n"
      "v_add_u32 v8, 1, v8\n"
      "v_cmp_gt_u32 s[96:97], s62, v8\n"
      "v_add_u32 v8, 1, v8\n"
      "v_cmp_gt_u32 s[98:99], s62, v8\n"
      "v_add_u32 v8, 1, v8\n"
      "v_cmp_gt_u32 s[100:101], s62, v8\n"
      "s_lshl_b32 s62, %[wv], 8\n"
      "s_add_u32 s62, s62, 155648\n"
      "v_lshrrev_b32 v8, 6, v240\n"
      "v_lshl_add_u32 v235, v8, 4, s62\n"
      "v_lshl_add_u32 v236, v9, 5, s62\n"
      "v_lshlrev_b32 v237, 5, v9\n"
      "v_add_u32 v237, 159744, v237\n"
      "v_lshrrev_b32 v238, 4, v240\n"
      "s_mov_b32 s40, %[olo]\n"
      "s_mov_b32 s41, %[ohi]\n"
      "s_mov_b32 s42, 4088320\n"
      "s_mov_b32 s56, 0x3ef62fad\n"
      "s_mov_b32 s57, 0xbfacc7ff\n"
      "s_mov_b32 s62, 0x3cb6d4dc\n"
      "s_mov_b32 s63, 0xc05ac6e7\n"
      "s_mov_b32 s64, 0xc0148900\n"
      "s_mov_b32 s65, 0x3b7deebf\n"
      "s_mov_b32 s66, 0xbe0b3e94\n"
      "s_mov_b32 s67, 0x3cc16524\n"
      "s_mov_b32 s68, 0x3e6623e0\n"
      "s_mov_b32 s69, 0xc0be2000\n"
      "s_mov_b32 s70, 0xbdd1af8e\n"
      "s_mov_b32 s71, 0x3e425f6a\n"
      "s_mov_b32 s72, 0x3e7e9e51\n"
      "s_mov_b32 s74, 0x3eec0625\n"
      "s_mov_b32 s76, 0x3c127a0c\n"
      "s_mov_b32 s77, 0xbbe24cb2\n"
      "s_mov_b32 s78, 0xbc500587\n"
      "s_mov_b32 s79, 0xbc20b115\n"
      "s_mov_b32 s83, 0x3f134a29\n"
      "s_mov_b32 s84, 0x4023d70a\n"
      "s_mov_b32 s85, 0x3ec80000\n"
      "s_mov_b32 s44, 0x3fde7920\n"
      "s_mov_b32 s45, 0xba2af53a\n"
      "s_mov_b32 s46, 0xbb3e2af0\n"
      "s_mov_b32 s47, 0x3f303b34\n"
      "s_mov_b32 s48, 0xbca9f9e4\n"
      "s_mov_b32 s49, 0xbfaaeb65\n"
      "s_mov_b32 s50, 0x408e6200\n"
      "s_cmp_lg_u32 %[wv], 4\n"
      "s_cbranch_scc1 LEND%=\n"
      "v_mov_b32 v8, 163776\n"
      "ds_read_b64 v[208:209], v8\n"
      "v_and_b32 v8, 31, v238\n"
      "v_and_b32 v9, 24, v8\n"
      "v_and_b32 v10, 3, v8\n"
      "v_lshl_add_u32 v9, v10, 1, v9\n"
      "v_bfe_u32 v10, v8, 2, 1\n"
      "v_add_u32 v9, v9, v10\n"
      "s_lshl_b32 s86, %[cb], 5\n"
      "v_add_u32 v9, s86, v9\n"
      "v_lshlrev_b32 v9, 1, v9\n"
      "global_load_ushort v200, v9, %[gam]\n"
      "global_load_ushort v201, v9, %[bet]\n"
      "v_lshrrev_b32 v11, 5, v238\n"
      "v_lshrrev_b32 v12, 3, v8\n"
      "v_lshlrev_b32 v12, 6, v12\n"
      "v_and_b32 v13, 7, v8\n"
      "v_lshl_add_u32 v12, v13, 2, v12\n"
      "v_lshl_add_u32 v12, v11, 10, v12\n"
      "v_add_u32 v202, 155648, v12\n"
      "v_lshlrev_b32 v12, 2, v8\n"
      "v_add_u32 v213, 157696, v12\n"
      "v_lshl_add_u32 v203, v11, 10, v213\n"
      "s_movk_i32 s86, 500\n"
      "s_cmp_eq_u32 %[rt], 15\n"
      "s_cselect_b32 s86, 485, s86\n"
      "s_sub_u32 s86, s86, 480\n"
      "v_lshlrev_b32 v12, 4, v11\n"
      "v_sub_u32 v12, s86, v12\n"
      "v_subrev_u32 v13, 0, v12\n"
      "v_med3_i32 v13, v13, 0, 4\n"
      "v_add_u32 v13, 60, v13\n"
      "v_cvt_f32_u32 v204, v13\n"
      "v_subrev_u32 v13, 4, v12\n"
      "v_med3_i32 v13, v13, 0, 4\n"
      "v_add_u32 v13, 60, v13\n"
      "v_cvt_f32_u32 v205, v13\n"
      "v_subrev_u32 v13, 8, v12\n"
      "v_med3_i32 v13, v13, 0, 4\n"
      "v_add_u32 v13, 60, v13\n"
      "v_cvt_f32_u32 v206, v13\n"
      "v_subrev_u32 v13, 12, v12\n"
      "v_med3_i32 v13, v13, 0, 4\n"
      "v_add_u32 v13, 60, v13\n"
      "v_cvt_f32_u32 v207, v13\n"
      "s_lshl_b32 s86, %[rt], 9\n"
      "v_lshl_add_u32 v210, v8, 4, s86\n"
      "v_lshlrev_b32 v211, 4, v238\n"
      "v_mov_b32 v212, 0x43fa0000\n"
      "v_mov_b32 v13, 0x43f28000\n"
      "v_cmp_lt_u32 vcc, 31, v238\n"
      "s_nop 1\n"
      "v_cndmask_b32 v212, v212, v13, vcc\n"
      "s_mov_b32 s92, 0x3b03126f\n"
      "s_cmp_eq_u32 %[rt], 15\n"
      "s_cselect_b32 s92, 0x3b072033, s92\n"
      "s_mov_b32 s88, %[rlo]\n"
      "s_mov_b32 s89, %[rhi]\n"
      "s_mov_b32 s90, 8192\n"
      "s_mov_b32 s91, 0x20000\n"
      "v_lshlrev_b32 v214, 2, v238\n"
      "v_add_u32 v214, 159744, v214\n"
      "s_waitcnt lgkmcnt(0)\n"
      "LEND%=:\n"
      "s_barrier\n"
      "ds_read_b128 v[44:47], v228 offset:0\n"
      "ds_read_b128 v[48:51], v231 offset:0\n"
      "ds_read_b128 v[52:55], v228 offset:16384\n"
      "ds_read_b128 v[56:59], v231 offset:16384\n"
      "ds_read_b128 v[60:63], v228 offset:32768\n"
      "ds_read_b128 v[64:67], v231 offset:32768\n"
      "ds_read_b128 v[68:71], v228 offset:49152\n"
      "ds_read_b128 v[72:75], v231 offset:49152\n"
      "ds_read_b128 v[100:103], v228 offset:1024\n"
      "ds_read_b128 v[104:107], v231 offset:1024\n"
      "ds_read_b128 v[108:111], v228 offset:17408\n"
      "ds_read_b128 v[112:115], v231 offset:17408\n"
      "ds_read_b128 v[116:119], v228 offset:33792\n"
      "ds_read_b128 v[120:123], v231 offset:33792\n"
      "s_waitcnt lgkmcnt(13)\n"
      "v_mul_f32 v12, s56, v44\n"
      "v_mul_f32 v13, s56, v45\n"
      "v_mul_f32 v14, s56, v46\n"
      "v_mul_f32 v15, s56, v47\n"
      "v_mul_f32 v20, s66, v44\n"
      "v_mul_f32 v21, s66, v45\n"
      "v_mul_f32 v22, s66, v46\n"
      "v_mul_f32 v23, s66, v47\n"
      "v_mul_f32 v28, s70, v44\n"
      "v_mul_f32 v29, s70, v45\n"
      "v_mul_f32 v30, s70, v46\n"
      "v_mul_f32 v31, s70, v47\n"
      "ds_read_b128 v[124:127], v228 offset:50176\n"
      "s_waitcnt lgkmcnt(13)\n"
      "v_fmac_f32 v28, s71, v48\n"
      "v_fmac_f32 v29, s71, v49\n"
      "v_fmac_f32 v30, s71, v50\n"
      "v_fmac_f32 v31, s71, v51\n"
      "v_mul_f32 v36, s47, v48\n"
      "v_mul_f32 v37, s47, v49\n"
      "v_mul_f32 v38, s47, v50\n"
      "v_mul_f32 v39, s47, v51\n"
      "ds_read_b128 v[128:131], v231 offset:50176\n"
      "s_waitcnt lgkmcnt(13)\n"
      "v_fmac_f32 v12, s57, v52\n"
      "v_fmac_f32 v13, s57, v53\n"
      "v_fmac_f32 v14, s57, v54\n"
      "v_fmac_f32 v15, s57, v55\n"
      "v_fmac_f32 v20, s66, v52\n"
      "v_fmac_f32 v21, s66, v53\n"
      "v_fmac_f32 v22, s66, v54\n"
      "v_fmac_f32 v23, s66, v55\n"
      "v_fmac_f32 v28, s72, v52\n"
      "v_fmac_f32 v29, s72, v53\n"
      "v_fmac_f32 v30, s72, v54\n"
      "v_fmac_f32 v31, s72, v55\n"
      "s_waitcnt lgkmcnt(12)\n"
      "v_fmac_f32 v28, s74, v56\n"
      "v_fmac_f32 v29, s74, v57\n"
      "v_fmac_f32 v30, s74, v58\n"
      "v_fmac_f32 v31, s74, v59\n"
      "v_fmac_f32 v36, s47, v56\n"
      "v_fmac_f32 v37, s47, v57\n"
      "v_fmac_f32 v38, s47, v58\n"
      "v_fmac_f32 v39, s47, v59\n"
      "s_waitcnt lgkmcnt(11)\n"
      "v_fmac_f32 v20, s67, v60\n"
      "v_fmac_f32 v21, s67, v61\n"
      "v_fmac_f32 v22, s67, v62\n"
      "v_fmac_f32 v23, s67, v63\n"
      "v_fmac_f32 v28, s76, v60\n"
      "v_fmac_f32 v29, s76, v61\n"
      "v_fmac_f32 v30, s76, v62\n"
      "v_fmac_f32 v31, s76, v63\n"
      "s_waitcnt lgkmcnt(10)\n"
      "v_fmac_f32 v28, s77, v64\n"
      "v_fmac_f32 v29, s77, v65\n"
      "v_fmac_f32 v30, s77, v66\n"
      "v_fmac_f32 v31, s77, v67\n"
      "v_fmac_f32 v36, s48, v64\n"
      "v_fmac_f32 v37, s48, v65\n"
      "v_fmac_f32 v38, s48, v66\n"
      "v_fmac_f32 v39, s48, v67\n"
      "s_waitcnt lgkmcnt(9)\n"
      "v_fmac_f32 v12, s62, v68\n"
      "v_fmac_f32 v13, s62, v69\n"
      "v_fmac_f32 v14, s62, v70\n"
      "v_fmac_f32 v15, s62, v71\n"
      "v_fmac_f32 v20, s67, v68\n"
      "v_fmac_f32 v21, s67, v69\n"
      "v_fmac_f32 v22, s67, v70\n"
      "v_fmac_f32 v23, s67, v71\n"
      "v_fmac_f32 v28, s78, v68\n"
      "v_fmac_f32 v29, s78, v69\n"
      "v_fmac_f32 v30, s78, v70\n"
      "v_fmac_f32 v31, s78, v71\n"
      "s_waitcnt lgkmcnt(8)\n"
      "v_fmac_f32 v28, s79, v72\n"
      "v_fmac_f32 v29, s79, v73\n"
      "v_fmac_f32 v30, s79, v74\n"
      "v_fmac_f32 v31, s79, v75\n"
      "v_fmac_f32 v36, s48, v72\n"
      "v_fmac_f32 v37, s48, v73\n"
      "v_fmac_f32 v38, s48, v74\n"
      "v_fmac_f32 v39, s48, v75\n"
      "s_waitcnt lgkmcnt(7)\n"
      "v_pk_mul_f32 v[16:17], v[100:101], s[56:57] op_sel:[0,0] op_sel_hi:[1,0]\n"
      "v_pk_mul_f32 v[18:19], v[102:103], s[56:57] op_sel:[0,0] op_sel_hi:[1,0]\n"
      "v_pk_mul_f32 v[24:25], v[100:101], s[66:67] op_sel:[0,0] op_sel_hi:[1,0]\n"
      "v_pk_mul_f32 v[26:27], v[102:103], s[66:67] op_sel:[0,0] op_sel_hi:[1,0]\n"
      "v_pk_mul_f32 v[32:33], v[100:101], s[70:71] op_sel:[0,0] op_sel_hi:[1,0]\n"
      "v_pk_mul_f32 v[34:35], v[102:103], s[70:71] op_sel:[0,0] op_sel_hi:[1,0]\n"
      "s_waitcnt lgkmcnt(6)\n"
      "v_pk_fma_f32 v[32:33], v[104:105], s[70:71], v[32:33] op_sel:[0,1,0] op_sel_hi:[1,1,1]\n"
      "v_pk_fma_f32 v[34:35], v[106:107], s[70:71], v[34:35] op_sel:[0,1,0] op_sel_hi:[1,1,1]\n"
      "v_pk_mul_f32 v[40:41], v[104:105], s[46:47] op_sel:[0,1] op_sel_hi:[1,1]\n"
      "v_pk_mul_f32 v[42:43], v[106:107], s[46:47] op_sel:[0,1] op_sel_hi:[1,1]\n"
      "s_waitcnt lgkmcnt(5)\n"
      "v_pk_fma_f32 v[16:17], v[108:109], s[56:57], v[16:17] op_sel:[0,1,0] op_sel_hi:[1,1,1]\n"
      "v_pk_fma_f32 v[18:19], v[110:111], s[56:57], v[18:19] op_sel:[0,1,0] op_sel_hi:[1,1,1]\n"
      "v_pk_fma_f32 v[24:25], v[108:109], s[66:67], v[24:25] op_sel:[0,0,0] op_sel_hi:[1,0,1]\n"
      "v_pk_fma_f32 v[26:27], v[110:111], s[66:67], v[26:27] op_sel:[0,0,0] op_sel_hi:[1,0,1]\n"
      "v_pk_fma_f32 v[32:33], v[108:109], s[72:73], v[32:33] op_sel:[0,0,0] op_sel_hi:[1,0,1]\n"
      "v_pk_fma_f32 v[34:35], v[110:111], s[72:73], v[34:35] op_sel:[0,0,0] op_sel_hi:[1,0,1]\n"
      "s_waitcnt lgkmcnt(4)\n"
      "v_pk_fma_f32 v[32:33], v[112:113], s[74:75], v[32:33] op_sel:[0,0,0] op_sel_hi:[1,0,1]\n"
      "v_pk_fma_f32 v[34:35], v[114:115], s[74:75], v[34:35] op_sel:[0,0,0] op_sel_hi:[1,0,1]\n"
      "v_pk_fma_f32 v[40:41], v[112:113], s[46:47], v[40:41] op_sel:[0,1,0] op_sel_hi:[1,1,1]\n"
      "v_pk_fma_f32 v[42:43], v[114:115], s[46:47], v[42:43] op_sel:[0,1,0] op_sel_hi:[1,1,1]\n"
      "s_waitcnt lgkmcnt(3)\n"
      "v_pk_fma_f32 v[24:25], v[116:117], s[66:67], v[24:25] op_sel:[0,1,0] op_sel_hi:[1,1,1]\n"
      "v_pk_fma_f32 v[26:27], v[118:119], s[66:67], v[26:27] op_sel:[0,1,0] op_sel_hi:[1,1,1]\n"
      "v_pk_fma_f32 v[32:33], v[116:117], s[76:77], v[32:33] op_sel:[0,0,0] op_sel_hi:[1,0,1]\n"
      "v_pk_fma_f32 v[34:35], v[118:119], s[76:77], v[34:35] op_sel:[0,0,0] op_sel_hi:[1,0,1]\n"
      "s_waitcnt lgkmcnt(2)\n"
      "v_pk_fma_f32 v[32:33], v[120:121], s[76:77], v[32:33] op_sel:[0,1,0] op_sel_hi:[1,1,1]\n"
      "v_pk_fma_f32 v[34:35], v[122:123], s[76:77], v[34:35] op_sel:[0,1,0] op_sel_hi:[1,1,1]\n"
      "v_pk_fma_f32 v[40:41], v[120:121], s[48:49], v[40:41] op_sel:[0,0,0] op_sel_hi:[1,0,1]\n"
      "v_pk_fma_f32 v[42:43], v[122:123], s[48:49], v[42:43] op_sel:[0,0,0] op_sel_hi:[1,0,1]\n"
      "s_waitcnt lgkmcnt(1)\n"
      "v_pk_fma_f32 v[16:17], v[124:125], s[62:63], v[16:17] op_sel:[0,0,0] op_sel_hi:[1,0,1]\n"
      "v_pk_fma_f32 v[18:19], v[126:127], s[62:63], v[18:19] op_sel:[0,0,0] op_sel_hi:[1,0,1]\n"
      "v_pk_fma_f32 v[24:25], v[124:125], s[66:67], v[24:25] op_sel:[0,1,0] op_sel_hi:[1,1,1]\n"
      "v_pk_fma_f32 v[26:27], v[126:127], s[66:67], v[26:27] op_sel:[0,1,0] op_sel_hi:[1,1,1]\n"
      "v_pk_fma_f32 v[32:33], v[124:125], s[78:79], v[32:33] op_sel:[0,0,0] op_sel_hi:[1,0,1]\n"
      "v_pk_fma_f32 v[34:35], v[126:127], s[78:79], v[34:35] op_sel:[0,0,0] op_sel_hi:[1,0,1]\n"
      "s_waitcnt lgkmcnt(0)\n"
      "v_pk_fma_f32 v[32:33], v[128:129], s[78:79], v[32:33] op_sel:[0,1,0] op_sel_hi:[1,1,1]\n"
      "v_pk_fma_f32 v[34:35], v[130:131], s[78:79], v[34:35] op_sel:[0,1,0] op_sel_hi:[1,1,1]\n"
      "v_pk_fma_f32 v[40:41], v[128:129], s[48:49], v[40:41] op_sel:[0,0,0] op_sel_hi:[1,0,1]\n"
      "v_pk_fma_f32 v[42:43], v[130:131], s[48:49], v[42:43] op_sel:[0,0,0] op_sel_hi:[1,0,1]\n"
      "s_branch LEND2%=\n"
      "LFB%=:\n"
      "v_mfma_f32_16x16x32_bf16 v[12:15], v[124:127], v[188:191], v[12:15]\n"
      "s_waitcnt vmcnt(9) lgkmcnt(7)\n"
      "v_mfma_f32_16x16x32_bf16 v[16:19], v[128:131], v[188:191], v[16:19]\n"
      "ds_read_b128 v[188:191], v1 offset:1024\n"
      "v_mfma_f32_16x16x32_bf16 v[20:23], v[124:127], v[192:195], v[20:23]\n"
      "v_mfma_f32_16x16x32_bf16 v[24:27], v[128:131], v[192:195], v[24:27]\n"
      "v_mfma_f32_16x16x32_bf16 v[12:15], v[132:135], v[192:195], v[12:15]\n"
      "s_waitcnt vmcnt(7) lgkmcnt(7)\n"
      "v_mfma_f32_16x16x32_bf16 v[16:19], v[136:139], v[192:195], v[16:19]\n"
      "ds_read_b128 v[192:195], v1 offset:2048\n"
      "v_mfma_f32_16x16x32_bf16 v[28:31], v[124:127], v[196:199], v[28:31]\n"
      "v_mfma_f32_16x16x32_bf16 v[32:35], v[128:131], v[196:199], v[32:35]\n"
      "buffer_load_dwordx4 v[172:175], v4, s[44:47], s52 offen offset:0 sc1\n"
      "v_mfma_f32_16x16x32_bf16 v[20:23], v[132:135], v[196:199], v[20:23]\n"
      "v_mfma_f32_16x16x32_bf16 v[24:27], v[136:139], v[196:199], v[24:27]\n"
      "v_mfma_f32_16x16x32_bf16 v[12:15], v[140:143], v[196:199], v[12:15]\n"
      "s_waitcnt vmcnt(6) lgkmcnt(7)\n"
      "v_mfma_f32_16x16x32_bf16 v[16:19], v[144:147], v[196:199], v[16:19]\n"
      "ds_read_b128 v[196:199], v2 offset:0\n"
      "v_mfma_f32_16x16x32_bf16 v[36:39], v[124:127], v[200:203], v[36:39]\n"
      "buffer_load_dwordx4 v[176:179], v4, s[44:47], s52 offen offset:1024 sc1\n"
      "v_mfma_f32_16x16x32_bf16 v[40:43], v[128:131], v[200:203], v[40:43]\n"
      "v_mfma_f32_16x16x32_bf16 v[28:31], v[132:135], v[200:203], v[28:31]\n"
      "v_mfma_f32_16x16x32_bf16 v[32:35], v[136:139], v[200:203], v[32:35]\n"
      "v_mfma_f32_16x16x32_bf16 v[20:23], v[140:143], v[200:203], v[20:23]\n"
      "buffer_load_dwordx4 v[180:183], v4, s[44:47], s52 offen offset:2048 sc1\n"
      "v_mfma_f32_16x16x32_bf16 v[24:27], v[144:147], v[200:203], v[24:27]\n"
      "v_mfma_f32_16x16x32_bf16 v[12:15], v[148:151], v[200:203], v[12:15]\n"
      "s_waitcnt vmcnt(7) lgkmcnt(7)\n"
      "v_mfma_f32_16x16x32_bf16 v[16:19], v[152:155], v[200:203], v[16:19]\n"
      "ds_read_b128 v[200:203], v2 offset:1024\n"
      "v_mfma_f32_16x16x32_bf16 v[44:47], v[124:127], v[204:207], v[44:47]\n"
      "v_mfma_f32_16x16x32_bf16 v[48:51], v[128:131], v[204:207], v[48:51]\n"
      "v_mfma_f32_16x16x32_bf16 v[36:39], v[132:135], v[204:207], v[36:39]\n"
      "buffer_load_dwordx4 v[184:187], v4, s[44:47], s52 offen offset:3072 sc1\n"
      "v_mfma_f32_16x16x32_bf16 v[40:43], v[136:139], v[204:207], v[40:43]\n"
      "ds_write_b128 v242, v[12:15] offset:0\n"
      "v_mfma_f32_16x16x32_bf16 v[28:31], v[140:143], v[204:207], v[28:31]\n"
      "ds_write_b128 v242, v[16:19] offset:1024\n"
      "v_mfma_f32_16x16x32_bf16 v[32:35], v[144:147], v[204:207], v[32:35]\n"
      "s_mov_b32 s40, %[olo]\n"
      "s_mov_b32 s41, %[ohi]\n"
      "v_mfma_f32_16x16x32_bf16 v[20:23], v[148:151], v[204:207], v[20:23]\n"
      "s_waitcnt lgkmcnt(9)\n"
      "s_mov_b32 s42, 4088320\n"
      "v_mfma_f32_16x16x32_bf16 v[24:27], v[152:155], v[204:207], v[24:27]\n"
      "ds_read_b128 v[204:207], v2 offset:2048\n"
      "s_mov_b32 s44, 0x3fde7920\n"
      "v_mfma_f32_16x16x32_bf16 v[52:55], v[124:127], v[208:211], v[52:55]\n"
      "s_mov_b32 s45, 0xba2af53a\n"
      "s_mov_b32 s46, 0xbb3e2af0\n"
      "v_mfma_f32_16x16x32_bf16 v[56:59], v[128:131], v[208:211], v[56:59]\n"
      "ds_write_b128 v242, v[20:23] offset:2048\n"
      "v_mfma_f32_16x16x32_bf16 v[44:47], v[132:135], v[208:211], v[44:47]\n"
      "ds_write_b128 v242, v[24:27] offset:3072\n"
      "v_mfma_f32_16x16x32_bf16 v[48:51], v[136:139], v[208:211], v[48:51]\n"
      "s_mov_b32 s47, 0x3f303b34\n"
      "s_mov_b32 s48, 0xbca9f9e4\n"
      "v_mfma_f32_16x16x32_bf16 v[36:39], v[140:143], v[208:211], v[36:39]\n"
      "s_mov_b32 s49, 0xbfaaeb65\n"
      "s_mov_b32 s50, 0x408e6200\n"
      "v_mfma_f32_16x16x32_bf16 v[40:43], v[144:147], v[208:211], v[40:43]\n"
      "v_mfma_f32_16x16x32_bf16 v[28:31], v[148:151], v[208:211], v[28:31]\n"
      "s_waitcnt lgkmcnt(11)\n"
      "v_mfma_f32_16x16x32_bf16 v[32:35], v[152:155], v[208:211], v[32:35]\n"
      "ds_read_b128 v[208:211], v2 offset:3072\n"
      "v_mfma_f32_16x16x32_bf16 v[60:63], v[124:127], v[216:219], v[60:63]\n"
      "ds_read_b128 v[212:215], v2 offset:4096\n"
      "v_mfma_f32_16x16x32_bf16 v[64:67], v[128:131], v[216:219], v[64:67]\n"
      "v_mfma_f32_16x16x32_bf16 v[52:55], v[132:135], v[216:219], v[52:55]\n"
      "v_mfma_f32_16x16x32_bf16 v[56:59], v[136:139], v[216:219], v[56:59]\n"
      "ds_write_b128 v242, v[28:31] offset:4096\n"
      "v_mfma_f32_16x16x32_bf16 v[44:47], v[140:143], v[216:219], v[44:47]\n"
      "ds_write_b128 v242, v[32:35] offset:5120\n"
      "v_mfma_f32_16x16x32_bf16 v[48:51], v[144:147], v[216:219], v[48:51]\n"
      "v_mfma_f32_16x16x32_bf16 v[36:39], v[148:151], v[216:219], v[36:39]\n"
      "s_waitcnt lgkmcnt(14)\n"
      "v_mfma_f32_16x16x32_bf16 v[40:43], v[152:155], v[216:219], v[40:43]\n"
      "ds_read_b128 v[216:219], v2 offset:5120\n"
      "v_mfma_f32_16x16x32_bf16 v[68:71], v[124:127], v[220:223], v[68:71]\n"
      "v_mfma_f32_16x16x32_bf16 v[72:75], v[128:131], v[220:223], v[72:75]\n"
      "v_mfma_f32_16x16x32_bf16 v[60:63], v[132:135], v[220:223], v[60:63]\n"
      "v_mfma_f32_16x16x32_bf16 v[64:67], v[136:139], v[220:223], v[64:67]\n"
      "v_mfma_f32_16x16x32_bf16 v[52:55], v[140:143], v[220:223], v[52:55]\n"
      "ds_write_b128 v242, v[36:39] offset:6144\n"
      "v_mfma_f32_16x16x32_bf16 v[56:59], v[144:147], v[220:223], v[56:59]\n"
      "ds_write_b128 v242, v[40:43] offset:7168\n"
      "v_mfma_f32_16x16x32_bf16 v[44:47], v[148:151], v[220:223], v[44:47]\n"
      "s_waitcnt lgkmcnt(15)\n"
      "v_mfma_f32_16x16x32_bf16 v[48:51], v[152:155], v[220:223], v[48:51]\n"
      "ds_read_b128 v[220:223], v2 offset:6144\n"
      "v_mfma_f32_16x16x32_bf16 v[68:71], v[132:135], v[224:227], v[68:71]\n"
      "v_mfma_f32_16x16x32_bf16 v[72:75], v[136:139], v[224:227], v[72:75]\n"
      "v_mfma_f32_16x16x32_bf16 v[60:63], v[140:143], v[224:227], v[60:63]\n"
      "v_mfma_f32_16x16x32_bf16 v[64:67], v[144:147], v[224:227], v[64:67]\n"
      "v_mfma_f32_16x16x32_bf16 v[52:55], v[148:151], v[224:227], v[52:55]\n"
      "s_waitcnt lgkmcnt(15)\n"
      "v_mfma_f32_16x16x32_bf16 v[56:59], v[152:155], v[224:227], v[56:59]\n"
      "ds_read_b128 v[224:227], v2 offset:7168\n"
      "v_mfma_f32_16x16x32_bf16 v[68:71], v[140:143], v[188:191], v[68:71]\n"
      "ds_write_b128 v242, v[44:47] offset:8192\n"
      "v_mfma_f32_16x16x32_bf16 v[72:75], v[144:147], v[188:191], v[72:75]\n"
      "ds_write_b128 v242, v[48:51] offset:9216\n"
      "v_mfma_f32_16x16x32_bf16 v[60:63], v[148:151], v[188:191], v[60:63]\n"
      "s_waitcnt lgkmcnt(15)\n"
      "v_mfma_f32_16x16x32_bf16 v[64:67], v[152:155], v[188:191], v[64:67]\n"
      "ds_read_b128 v[188:191], v3 offset:0\n"
      "v_mfma_f32_16x16x32_bf16 v[68:71], v[148:151], v[192:195], v[68:71]\n"
      "s_waitcnt vmcnt(7) lgkmcnt(15)\n"
      "v_mfma_f32_16x16x32_bf16 v[72:75], v[152:155], v[192:195], v[72:75]\n"
      "ds_read_b128 v[192:195], v3 offset:1024\n"
      "v_mfma_f32_16x16x32_bf16 v[76:79], v[156:159], v[196:199], v[76:79]\n"
      "ds_read_b128 v[196:199], v3 offset:2048\n"
      "s_waitcnt lgkmcnt(15)\n"
      "v_mfma_f32_16x16x32_bf16 v[80:83], v[156:159], v[200:203], v[80:83]\n"
      "s_waitcnt vmcnt(6) lgkmcnt(15)\n"
      "ds_write_b128 v242, v[52:55] offset:10240\n"
      "v_mfma_f32_16x16x32_bf16 v[76:79], v[160:163], v[200:203], v[76:79]\n"
      "ds_read_b128 v[200:203], v5 offset:0\n"
      "ds_write_b128 v242, v[56:59] offset:11264\n"
      "v_mfma_f32_16x16x32_bf16 v[84:87], v[156:159], v[204:207], v[84:87]\n"
      "ds_write_b128 v242, v[60:63] offset:12288\n"
      "v_mfma_f32_16x16x32_bf16 v[80:83], v[160:163], v[204:207], v[80:83]\n"
      "s_waitcnt vmcnt(5) lgkmcnt(15)\n"
      "ds_write_b128 v242, v[64:67] offset:13312\n"
      "v_mfma_f32_16x16x32_bf16 v[76:79], v[164:167], v[204:207], v[76:79]\n"
      "ds_read_b128 v[204:207], v5 offset:1024\n"
      "ds_write_b128 v242, v[68:71] offset:14336\n"
      "v_mfma_f32_16x16x32_bf16 v[88:91], v[156:159], v[208:211], v[88:91]\n"
      "ds_write_b128 v242, v[72:75] offset:15360\n"
      "v_mfma_f32_16x16x32_bf16 v[84:87], v[160:163], v[208:211], v[84:87]\n"
      "v_mfma_f32_16x16x32_bf16 v[80:83], v[164:167], v[208:211], v[80:83]\n"
      "s_waitcnt vmcnt(4) lgkmcnt(15)\n"
      "v_mfma_f32_16x16x32_bf16 v[76:79], v[168:171], v[208:211], v[76:79]\n"
      "ds_read_b128 v[208:211], v5 offset:2048\n"
      "v_mfma_f32_16x16x32_bf16 v[92:95], v[156:159], v[212:215], v[92:95]\n"
      "v_mfma_f32_16x16x32_bf16 v[88:91], v[160:163], v[212:215], v[88:91]\n"
      "s_waitcnt lgkmcnt(1)\n"
      "s_barrier\n"
      "v_mfma_f32_16x16x32_bf16 v[84:87], v[164:167], v[212:215], v[84:87]\n"
      "s_waitcnt lgkmcnt(15)\n"
      "v_mfma_f32_16x16x32_bf16 v[80:83], v[168:171], v[212:215], v[80:83]\n"
      "ds_read_b128 v[212:215], v5 offset:3072\n"
      "ds_write_b128 v243, v[76:79] offset:0\n"
      "v_mfma_f32_16x16x32_bf16 v[96:99], v[156:159], v[216:219], v[96:99]\n"
      "v_mfma_f32_16x16x32_bf16 v[92:95], v[160:163], v[216:219], v[92:95]\n"
      "v_mfma_f32_16x16x32_bf16 v[88:91], v[164:167], v[216:219], v[88:91]\n"
      "s_waitcnt lgkmcnt(15)\n"
      "v_mfma_f32_16x16x32_bf16 v[84:87], v[168:171], v[216:219], v[84:87]\n"
      "ds_read_b128 v[216:219], v6 offset:0\n"
      "ds_write_b128 v243, v[80:83] offset:2048\n"
      "v_mfma_f32_16x16x32_bf16 v[100:103], v[156:159], v[220:223], v[100:103]\n"
      "v_mfma_f32_16x16x32_bf16 v[96:99], v[160:163], v[220:223], v[96:99]\n"
      "v_mfma_f32_16x16x32_bf16 v[92:95], v[164:167], v[220:223], v[92:95]\n"
      "s_waitcnt lgkmcnt(15)\n"
      "v_mfma_f32_16x16x32_bf16 v[88:91], v[168:171], v[220:223], v[88:91]\n"
      "ds_read_b128 v[220:223], v6 offset:1024\n"
      "ds_write_b128 v243, v[84:87] offset:4096\n"
      "v_mfma_f32_16x16x32_bf16 v[104:107], v[156:159], v[224:227], v[104:107]\n"
      "v_mfma_f32_16x16x32_bf16 v[100:103], v[160:163], v[224:227], v[100:103]\n"
      "v_mfma_f32_16x16x32_bf16 v[96:99], v[164:167], v[224:227], v[96:99]\n"
      "s_waitcnt lgkmcnt(15)\n"
      "v_mfma_f32_16x16x32_bf16 v[92:95], v[168:171], v[224:227], v[92:95]\n"
      "ds_read_b128 v[224:227], v6 offset:2048\n"
      "ds_write_b128 v243, v[88:91] offset:6144\n"
      "v_mfma_f32_16x16x32_bf16 v[104:107], v[160:163], v[188:191], v[104:107]\n"
      "v_mfma_f32_16x16x32_bf16 v[100:103], v[164:167], v[188:191], v[100:103]\n"
      "v_mfma_f32_16x16x32_bf16 v[96:99], v[168:171], v[188:191], v[96:99]\n"
      "s_waitcnt lgkmcnt(15)\n"
      "v_mfma_f32_16x16x32_bf16 v[104:107], v[164:167], v[192:195], v[104:107]\n"
      "v_mfma_f32_16x16x32_bf16 v[100:103], v[168:171], v[192:195], v[100:103]\n"
      "s_waitcnt lgkmcnt(15)\n"
      "ds_write_b128 v243, v[92:95] offset:8192\n"
      "v_mfma_f32_16x16x32_bf16 v[104:107], v[168:171], v[196:199], v[104:107]\n"
      "s_waitcnt vmcnt(3) lgkmcnt(15)\n"
      "v_mfma_f32_16x16x32_bf16 v[108:111], v[172:175], v[200:203], v[108:111]\n"
      "s_waitcnt lgkmcnt(12)\n"
      "ds_write_b128 v243, v[96:99] offset:10240\n"
      "v_mfma_f32_16x16x32_bf16 v[112:115], v[172:175], v[204:207], v[112:115]\n"
      "s_waitcnt vmcnt(0) lgkmcnt(3)\n"
      "ds_write_b128 v243, v[100:103] offset:12288\n"
      "v_mfma_f32_16x16x32_bf16 v[108:111], v[176:179], v[204:207], v[108:111]\n"
      "ds_write_b128 v243, v[104:107] offset:14336\n"
      "v_mfma_f32_16x16x32_bf16 v[116:119], v[172:175], v[208:211], v[116:119]\n"
      "v_mfma_f32_16x16x32_bf16 v[112:115], v[176:179], v[208:211], v[112:115]\n"
      "v_mfma_f32_16x16x32_bf16 v[108:111], v[180:183], v[208:211], v[108:111]\n"
      "v_mfma_f32_16x16x32_bf16 v[120:123], v[172:175], v[212:215], v[120:123]\n"
      "v_mfma_f32_16x16x32_bf16 v[116:119], v[176:179], v[212:215], v[116:119]\n"
      "v_mfma_f32_16x16x32_bf16 v[112:115], v[180:183], v[212:215], v[112:115]\n"
      "v_mfma_f32_16x16x32_bf16 v[108:111], v[184:187], v[212:215], v[108:111]\n"
      "v_mfma_f32_16x16x32_bf16 v[120:123], v[176:179], v[216:219], v[120:123]\n"
      "v_mfma_f32_16x16x32_bf16 v[116:119], v[180:183], v[216:219], v[116:119]\n"
      "v_mfma_f32_16x16x32_bf16 v[112:115], v[184:187], v[216:219], v[112:115]\n"
      "v_mfma_f32_16x16x32_bf16 v[120:123], v[180:183], v[220:223], v[120:123]\n"
      "v_mfma_f32_16x16x32_bf16 v[116:119], v[184:187], v[220:223], v[116:119]\n"
      "v_mfma_f32_16x16x32_bf16 v[120:123], v[184:187], v[224:227], v[120:123]\n"
      "s_nop 0\n"
      "s_nop 0\n"
      "ds_write_b128 v244, v[108:111] offset:0\n"
      "s_nop 0\n"
      "s_nop 0\n"
      "ds_write_b128 v244, v[112:115] offset:1024\n"
      "s_nop 0\n"
      "ds_write_b128 v244, v[116:119] offset:2048\n"
      "ds_write_b128 v244, v[120:123] offset:3072\n"
      "s_setprio 0\n"
      "s_waitcnt lgkmcnt(0)\n"
      "ds_read_b128 v[44:47], v228 offset:0\n"
      "ds_read_b128 v[48:51], v231 offset:0\n"
      "ds_read_b128 v[52:55], v228 offset:16384\n"
      "ds_read_b128 v[56:59], v231 offset:16384\n"
      "ds_read_b128 v[60:63], v228 offset:32768\n"
      "ds_read_b128 v[64:67], v231 offset:32768\n"
      "ds_read_b128 v[68:71], v228 offset:49152\n"
      "ds_read_b128 v[72:75], v231 offset:49152\n"
      "ds_read_b128 v[100:103], v228 offset:1024\n"
      "ds_read_b128 v[104:107], v231 offset:1024\n"
      "ds_read_b128 v[108:111], v228 offset:17408\n"
      "ds_read_b128 v[112:115], v231 offset:17408\n"
      "ds_read_b128 v[116:119], v228 offset:33792\n"
      "ds_read_b128 v[120:123], v231 offset:33792\n"
      "s_waitcnt lgkmcnt(13)\n"
      "v_pk_mul_f32 v[12:13], v[44:45], s[56:57] op_sel:[0,0] op_sel_hi:[1,0]\n"
      "v_pk_mul_f32 v[14:15], v[46:47], s[56:57] op_sel:[0,0] op_sel_hi:[1,0]\n"
      "v_pk_mul_f32 v[20:21], v[44:45], s[66:67] op_sel:[0,0] op_sel_hi:[1,0]\n"
      "v_pk_mul_f32 v[22:23], v[46:47], s[66:67] op_sel:[0,0] op_sel_hi:[1,0]\n"
      "v_pk_mul_f32 v[28:29], v[44:45], s[70:71] op_sel:[0,0] op_sel_hi:[1,0]\n"
      "v_pk_mul_f32 v[30:31], v[46:47], s[70:71] op_sel:[0,0] op_sel_hi:[1,0]\n"
      "ds_read_b128 v[124:127], v228 offset:50176\n"
      "s_waitcnt lgkmcnt(13)\n"
      "v_pk_fma_f32 v[28:29], v[48:49], s[70:71], v[28:29] op_sel:[0,1,0] op_sel_hi:[1,1,1]\n"
      "v_pk_fma_f32 v[30:31], v[50:51], s[70:71], v[30:31] op_sel:[0,1,0] op_sel_hi:[1,1,1]\n"
      "v_pk_mul_f32 v[36:37], v[48:49], s[46:47] op_sel:[0,1] op_sel_hi:[1,1]\n"
      "v_pk_mul_f32 v[38:39], v[50:51], s[46:47] op_sel:[0,1] op_sel_hi:[1,1]\n"
      "ds_read_b128 v[128:131], v231 offset:50176\n"
      "s_waitcnt lgkmcnt(13)\n"
      "v_pk_fma_f32 v[12:13], v[52:53], s[56:57], v[12:13] op_sel:[0,1,0] op_sel_hi:[1,1,1]\n"
      "v_pk_fma_f32 v[14:15], v[54:55], s[56:57], v[14:15] op_sel:[0,1,0] op_sel_hi:[1,1,1]\n"
      "v_pk_fma_f32 v[20:21], v[52:53], s[66:67], v[20:21] op_sel:[0,0,0] op_sel_hi:[1,0,1]\n"
      "v_pk_fma_f32 v[22:23], v[54:55], s[66:67], v[22:23] op_sel:[0,0,0] op_sel_hi:[1,0,1]\n"
      "v_pk_fma_f32 v[28:29], v[52:53], s[72:73], v[28:29] op_sel:[0,0,0] op_sel_hi:[1,0,1]\n"
      "v_pk_fma_f32 v[30:31], v[54:55], s[72:73], v[30:31] op_sel:[0,0,0] op_sel_hi:[1,0,1]\n"
      "s_waitcnt lgkmcnt(12)\n"
      "v_pk_fma_f32 v[28:29], v[56:57], s[74:75], v[28:29] op_sel:[0,0,0] op_sel_hi:[1,0,1]\n"
      "v_pk_fma_f32 v[30:31], v[58:59], s[74:75], v[30:31] op_sel:[0,0,0] op_sel_hi:[1,0,1]\n"
      "v_pk_fma_f32 v[36:37], v[56:57], s[46:47], v[36:37] op_sel:[0,1,0] op_sel_hi:[1,1,1]\n"
      "v_pk_fma_f32 v[38:39], v[58:59], s[46:47], v[38:39] op_sel:[0,1,0] op_sel_hi:[1,1,1]\n"
      "s_waitcnt lgkmcnt(11)\n"
      "v_pk_fma_f32 v[20:21], v[60:61], s[66:67], v[20:21] op_sel:[0,1,0] op_sel_hi:[1,1,1]\n"
      "v_pk_fma_f32 v[22:23], v[62:63], s[66:67], v[22:23] op_sel:[0,1,0] op_sel_hi:[1,1,1]\n"
      "v_pk_fma_f32 v[28:29], v[60:61], s[76:77], v[28:29] op_sel:[0,0,0] op_sel_hi:[1,0,1]\n"
      "v_pk_fma_f32 v[30:31], v[62:63], s[76:77], v[30:31] op_sel:[0,0,0] op_sel_hi:[1,0,1]\n"
      "s_waitcnt lgkmcnt(10)\n"
      "v_pk_fma_f32 v[28:29], v[64:65], s[76:77], v[28:29] op_sel:[0,1,0] op_sel_hi:[1,1,1]\n"
      "v_pk_fma_f32 v[30:31], v[66:67], s[76:77], v[30:31] op_sel:[0,1,0] op_sel_hi:[1,1,1]\n"
      "v_pk_fma_f32 v[36:37], v[64:65], s[48:49], v[36:37] op_sel:[0,0,0] op_sel_hi:[1,0,1]\n"
      "v_pk_fma_f32 v[38:39], v[66:67], s[48:49], v[38:39] op_sel:[0,0,0] op_sel_hi:[1,0,1]\n"
      "s_waitcnt lgkmcnt(9)\n"
      "v_pk_fma_f32 v[12:13], v[68:69], s[62:63], v[12:13] op_sel:[0,0,0] op_sel_hi:[1,0,1]\n"
      "v_pk_fma_f32 v[14:15], v[70:71], s[62:63], v[14:15] op_sel:[0,0,0] op_sel_hi:[1,0,1]\n"
      "v_pk_fma_f32 v[20:21], v[68:69], s[66:67], v[20:21] op_sel:[0,1,0] op_sel_hi:[1,1,1]\n"
      "v_pk_fma_f32 v[22:23], v[70:71], s[66:67], v[22:23] op_sel:[0,1,0] op_sel_hi:[1,1,1]\n"
      "v_pk_fma_f32 v[28:29], v[68:69], s[78:79], v[28:29] op_sel:[0,0,0] op_sel_hi:[1,0,1]\n"
      "v_pk_fma_f32 v[30:31], v[70:71], s[78:79], v[30:31] op_sel:[0,0,0] op_sel_hi:[1,0,1]\n"
      "s_waitcnt lgkmcnt(8)\n"
      "v_pk_fma_f32 v[28:29], v[72:73], s[78:79], v[28:29] op_sel:[0,1,0] op_sel_hi:[1,1,1]\n"
      "v_pk_fma_f32 v[30:31], v[74:75], s[78:79], v[30:31] op_sel:[0,1,0] op_sel_hi:[1,1,1]\n"
      "v_pk_fma_f32 v[36:37], v[72:73], s[48:49], v[36:37] op_sel:[0,0,0] op_sel_hi:[1,0,1]\n"
      "v_pk_fma_f32 v[38:39], v[74:75], s[48:49], v[38:39] op_sel:[0,0,0] op_sel_hi:[1,0,1]\n"
      "s_waitcnt lgkmcnt(7)\n"
      "v_pk_mul_f32 v[16:17], v[100:101], s[56:57] op_sel:[0,0] op_sel_hi:[1,0]\n"
      "v_pk_mul_f32 v[18:19], v[102:103], s[56:57] op_sel:[0,0] op_sel_hi:[1,0]\n"
      "v_pk_mul_f32 v[24:25], v[100:101], s[66:67] op_sel:[0,0] op_sel_hi:[1,0]\n"
      "v_pk_mul_f32 v[26:27], v[102:103], s[66:67] op_sel:[0,0] op_sel_hi:[1,0]\n"
      "v_pk_mul_f32 v[32:33], v[100:101], s[70:71] op_sel:[0,0] op_sel_hi:[1,0]\n"
      "v_pk_mul_f32 v[34:35], v[102:103], s[70:71] op_sel:[0,0] op_sel_hi:[1,0]\n"
      "s_waitcnt lgkmcnt(6)\n"
      "v_pk_fma_f32 v[32:33], v[104:105], s[70:71], v[32:33] op_sel:[0,1,0] op_sel_hi:[1,1,1]\n"
      "v_pk_fma_f32 v[34:35], v[106:107], s[70:71], v[34:35] op_sel:[0,1,0] op_sel_hi:[1,1,1]\n"
      "v_pk_mul_f32 v[40:41], v[104:105], s[46:47] op_sel:[0,1] op_sel_hi:[1,1]\n"
      "v_pk_mul_f32 v[42:43], v[106:107], s[46:47] op_sel:[0,1] op_sel_hi:[1,1]\n"
      "s_waitcnt lgkmcnt(5)\n"
      "v_pk_fma_f32 v[16:17], v[108:109], s[56:57], v[16:17] op_sel:[0,1,0] op_sel_hi:[1,1,1]\n"
      "v_pk_fma_f32 v[18:19], v[110:111], s[56:57], v[18:19] op_sel:[0,1,0] op_sel_hi:[1,1,1]\n"
      "v_pk_fma_f32 v[24:25], v[108:109], s[66:67], v[24:25] op_sel:[0,0,0] op_sel_hi:[1,0,1]\n"
      "v_pk_fma_f32 v[26:27], v[110:111], s[66:67], v[26:27] op_sel:[0,0,0] op_sel_hi:[1,0,1]\n"
      "v_pk_fma_f32 v[32:33], v[108:109], s[72:73], v[32:33] op_sel:[0,0,0] op_sel_hi:[1,0,1]\n"
      "v_pk_fma_f32 v[34:35], v[110:111], s[72:73], v[34:35] op_sel:[0,0,0] op_sel_hi:[1,0,1]\n"
      "s_waitcnt lgkmcnt(4)\n"
      "v_pk_fma_f32 v[32:33], v[112:113], s[74:75], v[32:33] op_sel:[0,0,0] op_sel_hi:[1,0,1]\n"
      "v_pk_fma_f32 v[34:35], v[114:115], s[74:75], v[34:35] op_sel:[0,0,0] op_sel_hi:[1,0,1]\n"
      "v_pk_fma_f32 v[40:41], v[112:113], s[46:47], v[40:41] op_sel:[0,1,0] op_sel_hi:[1,1,1]\n"
      "v_pk_fma_f32 v[42:43], v[114:115], s[46:47], v[42:43] op_sel:[0,1,0] op_sel_hi:[1,1,1]\n"
      "s_waitcnt lgkmcnt(3)\n"
      "v_pk_fma_f32 v[24:25], v[116:117], s[66:67], v[24:25] op_sel:[0,1,0] op_sel_hi:[1,1,1]\n"
      "v_pk_fma_f32 v[26:27], v[118:119], s[66:67], v[26:27] op_sel:[0,1,0] op_sel_hi:[1,1,1]\n"
      "v_pk_fma_f32 v[32:33], v[116:117], s[76:77], v[32:33] op_sel:[0,0,0] op_sel_hi:[1,0,1]\n"
      "v_pk_fma_f32 v[34:35], v[118:119], s[76:77], v[34:35] op_sel:[0,0,0] op_sel_hi:[1,0,1]\n"
      "s_waitcnt lgkmcnt(2)\n"
      "v_pk_fma_f32 v[32:33], v[120:121], s[76:77], v[32:33] op_sel:[0,1,0] op_sel_hi:[1,1,1]\n"
      "v_pk_fma_f32 v[34:35], v[122:123], s[76:77], v[34:35] op_sel:[0,1,0] op_sel_hi:[1,1,1]\n"
      "v_pk_fma_f32 v[40:41], v[120:121], s[48:49], v[40:41] op_sel:[0,0,0] op_sel_hi:[1,0,1]\n"
      "v_pk_fma_f32 v[42:43], v[122:123], s[48:49], v[42:43] op_sel:[0,0,0] op_sel_hi:[1,0,1]\n"
      "s_waitcnt lgkmcnt(1)\n"
      "v_pk_fma_f32 v[16:17], v[124:125], s[62:63], v[16:17] op_sel:[0,0,0] op_sel_hi:[1,0,1]\n"
      "v_pk_fma_f32 v[18:19], v[126:127], s[62:63], v[18:19] op_sel:[0,0,0] op_sel_hi:[1,0,1]\n"
      "v_pk_fma_f32 v[24:25], v[124:125], s[66:67], v[24:25] op_sel:[0,1,0] op_sel_hi:[1,1,1]\n"
      "v_pk_fma_f32 v[26:27], v[126:127], s[66:67], v[26:27] op_sel:[0,1,0] op_sel_hi:[1,1,1]\n"
      "v_pk_fma_f32 v[32:33], v[124:125], s[78:79], v[32:33] op_sel:[0,0,0] op_sel_hi:[1,0,1]\n"
      "v_pk_fma_f32 v[34:35], v[126:127], s[78:79], v[34:35] op_sel:[0,0,0] op_sel_hi:[1,0,1]\n"
      "s_waitcnt lgkmcnt(0)\n"
      "v_pk_fma_f32 v[32:33], v[128:129], s[78:79], v[32:33] op_sel:[0,1,0] op_sel_hi:[1,1,1]\n"
      "v_pk_fma_f32 v[34:35], v[130:131], s[78:79], v[34:35] op_sel:[0,1,0] op_sel_hi:[1,1,1]\n"
      "v_pk_fma_f32 v[40:41], v[128:129], s[48:49], v[40:41] op_sel:[0,0,0] op_sel_hi:[1,0,1]\n"
      "v_pk_fma_f32 v[42:43], v[130:131], s[48:49], v[42:43] op_sel:[0,0,0] op_sel_hi:[1,0,1]\n"
      "LEND2%=:\n"
      "s_barrier\n"
      "ds_read_b128 v[76:79], v229 offset:0\n"
      "ds_read_b128 v[80:83], v232 offset:0\n"
      "ds_read_b128 v[84:87], v229 offset:16384\n"
      "ds_read_b128 v[88:91], v232 offset:16384\n"
      "ds_read_b128 v[92:95], v230 offset:0\n"
      "ds_read_b128 v[96:99], v233 offset:0\n"
      "ds_read_b128 v[132:135], v229 offset:1024\n"
      "ds_read_b128 v[136:139], v232 offset:1024\n"
      "ds_read_b128 v[140:143], v229 offset:17408\n"
      "ds_read_b128 v[144:147], v232 offset:17408\n"
      "ds_read_b128 v[148:151], v230 offset:8192\n"
      "ds_read_b128 v[152:155], v233 offset:8192\n"
      "s_waitcnt lgkmcnt(11)\n"
      "v_pk_fma_f32 v[12:13], v[76:77], s[62:63], v[12:13] op_sel:[0,1,0] op_sel_hi:[1,1,1]\n"
      "v_pk_fma_f32 v[14:15], v[78:79], s[62:63], v[14:15] op_sel:[0,1,0] op_sel_hi:[1,1,1]\n"
      "v_pk_fma_f32 v[20:21], v[76:77], s[68:69], v[20:21] op_sel:[0,0,0] op_sel_hi:[1,0,1]\n"
      "v_pk_fma_f32 v[22:23], v[78:79], s[68:69], v[22:23] op_sel:[0,0,0] op_sel_hi:[1,0,1]\n"
      "v_pk_fma_f32 v[28:29], v[76:77], s[82:83], v[28:29] op_sel:[0,1,0] op_sel_hi:[1,1,1]\n"
      "v_pk_fma_f32 v[30:31], v[78:79], s[82:83], v[30:31] op_sel:[0,1,0] op_sel_hi:[1,1,1]\n"
      "s_waitcnt lgkmcnt(10)\n"
      "v_pk_add_f32 v[20:21], v[20:21], v[80:81]\n"
      "v_pk_add_f32 v[22:23], v[22:23], v[82:83]\n"
      "v_pk_fma_f32 v[28:29], v[80:81], s[84:85], v[28:29] op_sel:[0,0,0] op_sel_hi:[1,0,1]\n"
      "v_pk_fma_f32 v[30:31], v[82:83], s[84:85], v[30:31] op_sel:[0,0,0] op_sel_hi:[1,0,1]\n"
      "v_pk_fma_f32 v[36:37], v[80:81], s[48:49], v[36:37] op_sel:[0,1,0] op_sel_hi:[1,1,1]\n"
      "v_pk_fma_f32 v[38:39], v[82:83], s[48:49], v[38:39] op_sel:[0,1,0] op_sel_hi:[1,1,1]\n"
      "s_waitcnt lgkmcnt(9)\n"
      "v_pk_fma_f32 v[12:13], v[84:85], s[64:65], v[12:13] op_sel:[0,0,0] op_sel_hi:[1,0,1]\n"
      "v_pk_fma_f32 v[14:15], v[86:87], s[64:65], v[14:15] op_sel:[0,0,0] op_sel_hi:[1,0,1]\n"
      "v_pk_fma_f32 v[20:21], v[84:85], s[68:69], v[20:21] op_sel:[0,1,0] op_sel_hi:[1,1,1]\n"
      "v_pk_fma_f32 v[22:23], v[86:87], s[68:69], v[22:23] op_sel:[0,1,0] op_sel_hi:[1,1,1]\n"
      "v_pk_fma_f32 v[28:29], v[84:85], s[84:85], v[28:29] op_sel:[0,1,0] op_sel_hi:[1,1,1]\n"
      "v_pk_fma_f32 v[30:31], v[86:87], s[84:85], v[30:31] op_sel:[0,1,0] op_sel_hi:[1,1,1]\n"
      "v_pk_add_f32 v[36:37], v[36:37], v[84:85]\n"
      "v_pk_add_f32 v[38:39], v[38:39], v[86:87]\n"
      "s_waitcnt lgkmcnt(8)\n"
      "v_pk_fma_f32 v[28:29], v[88:89], s[44:45], v[28:29] op_sel:[0,0,0] op_sel_hi:[1,0,1]\n"
      "v_pk_fma_f32 v[30:31], v[90:91], s[44:45], v[30:31] op_sel:[0,0,0] op_sel_hi:[1,0,1]\n"
      "v_pk_fma_f32 v[36:37], v[88:89], s[50:51], v[36:37] op_sel:[0,0,0] op_sel_hi:[1,0,1]\n"
      "v_pk_fma_f32 v[38:39], v[90:91], s[50:51], v[38:39] op_sel:[0,0,0] op_sel_hi:[1,0,1]\n"
      "s_waitcnt lgkmcnt(7)\n"
      "v_pk_fma_f32 v[12:13], v[92:93], s[64:65], v[12:13] op_sel:[0,1,0] op_sel_hi:[1,1,1]\n"
      "v_pk_fma_f32 v[14:15], v[94:95], s[64:65], v[14:15] op_sel:[0,1,0] op_sel_hi:[1,1,1]\n"
      "v_pk_fma_f32 v[28:29], v[92:93], s[44:45], v[28:29] op_sel:[0,1,0] op_sel_hi:[1,1,1]\n"
      "v_pk_fma_f32 v[30:31], v[94:95], s[44:45], v[30:31] op_sel:[0,1,0] op_sel_hi:[1,1,1]\n"
      "s_waitcnt lgkmcnt(6)\n"
      "v_pk_fma_f32 v[28:29], v[96:97], s[46:47], v[28:29] op_sel:[0,0,0] op_sel_hi:[1,0,1]\n"
      "v_pk_fma_f32 v[30:31], v[98:99], s[46:47], v[30:31] op_sel:[0,0,0] op_sel_hi:[1,0,1]\n"
      "s_nop 1\n"
      "v_mov_b32_dpp v160, v12 row_newbcast:0 row_mask:0xf bank_mask:0xf\n"
      "v_mov_b32_dpp v161, v13 row_newbcast:0 row_mask:0xf bank_mask:0xf\n"
      "v_mov_b32_dpp v162, v14 row_newbcast:0 row_mask:0xf bank_mask:0xf\n"
      "v_mov_b32_dpp v163, v15 row_newbcast:0 row_mask:0xf bank_mask:0xf\n"
      "v_pk_add_f32 v[168:169], v[12:13], v[160:161] neg_lo:[0,1] neg_hi:[0,1]\n"
      "v_pk_add_f32 v[170:171], v[14:15], v[162:163] neg_lo:[0,1] neg_hi:[0,1]\n"
      "v_pk_add_f32 v[172:173], v[20:21], v[160:161] neg_lo:[0,1] neg_hi:[0,1]\n"
      "v_pk_add_f32 v[174:175], v[22:23], v[162:163] neg_lo:[0,1] neg_hi:[0,1]\n"
      "v_pk_add_f32 v[176:177], v[28:29], v[160:161] neg_lo:[0,1] neg_hi:[0,1]\n"
      "v_pk_add_f32 v[178:179], v[30:31], v[162:163] neg_lo:[0,1] neg_hi:[0,1]\n"
      "v_pk_add_f32 v[180:181], v[36:37], v[160:161] neg_lo:[0,1] neg_hi:[0,1]\n"
      "v_pk_add_f32 v[182:183], v[38:39], v[162:163] neg_lo:[0,1] neg_hi:[0,1]\n"
      "s_cmp_eq_u64 s[100:101], -1\n"
      "s_cbranch_scc1 LMK0%=\n"
      "s_not_b64 exec, s[94:95]\n"
      "v_mov_b64 v[168:169], 0\n"
      "v_mov_b64 v[170:171], 0\n"
      "s_not_b64 exec, s[96:97]\n"
      "v_mov_b64 v[172:173], 0\n"
      "v_mov_b64 v[174:175], 0\n"
      "s_not_b64 exec, s[98:99]\n"
      "v_mov_b64 v[176:177], 0\n"
      "v_mov_b64 v[178:179], 0\n"
      "s_not_b64 exec, s[100:101]\n"
      "v_mov_b64 v[180:181], 0\n"
      "v_mov_b64 v[182:183], 0\n"
      "s_mov_b64 exec, -1\n"
      "LMK0%=:\n"
      "v_pk_add_f32 v[184:185], v[168:169], v[172:173]\n"
      "v_pk_add_f32 v[186:187], v[170:171], v[174:175]\n"
      "v_pk_mul_f32 v[192:193], v[168:169], v[168:169]\n"
      "v_pk_mul_f32 v[194:195], v[170:171], v[170:171]\n"
      "v_pk_add_f32 v[156:157], v[176:177], v[180:181]\n"
      "v_pk_add_f32 v[158:159], v[178:179], v[182:183]\n"
      "v_pk_fma_f32 v[192:193], v[172:173], v[172:173], v[192:193]\n"
      "v_pk_fma_f32 v[194:195], v[174:175], v[174:175], v[194:195]\n"
      "v_pk_add_f32 v[184:185], v[184:185], v[156:157]\n"
      "v_pk_add_f32 v[186:187], v[186:187], v[158:159]\n"
      "v_pk_fma_f32 v[192:193], v[176:177], v[176:177], v[192:193]\n"
      "v_pk_fma_f32 v[194:195], v[178:179], v[178:179], v[194:195]\n"
      "v_pk_fma_f32 v[192:193], v[180:181], v[180:181], v[192:193]\n"
      "v_pk_fma_f32 v[194:195], v[182:183], v[182:183], v[194:195]\n"
      "s_waitcnt lgkmcnt(5)\n"
      "v_pk_fma_f32 v[16:17], v[132:133], s[62:63], v[16:17] op_sel:[0,1,0] op_sel_hi:[1,1,1]\n"
      "v_pk_fma_f32 v[18:19], v[134:135], s[62:63], v[18:19] op_sel:[0,1,0] op_sel_hi:[1,1,1]\n"
      "v_pk_fma_f32 v[24:25], v[132:133], s[68:69], v[24:25] op_sel:[0,0,0] op_sel_hi:[1,0,1]\n"
      "v_pk_fma_f32 v[26:27], v[134:135], s[68:69], v[26:27] op_sel:[0,0,0] op_sel_hi:[1,0,1]\n"
      "v_pk_fma_f32 v[32:33], v[132:133], s[82:83], v[32:33] op_sel:[0,1,0] op_sel_hi:[1,1,1]\n"
      "v_pk_fma_f32 v[34:35], v[134:135], s[82:83], v[34:35] op_sel:[0,1,0] op_sel_hi:[1,1,1]\n"
      "s_waitcnt lgkmcnt(4)\n"
      "v_pk_add_f32 v[24:25], v[24:25], v[136:137]\n"
      "v_pk_add_f32 v[26:27], v[26:27], v[138:139]\n"
      "v_pk_fma_f32 v[32:33], v[136:137], s[84:85], v[32:33] op_sel:[0,0,0] op_sel_hi:[1,0,1]\n"
      "v_pk_fma_f32 v[34:35], v[138:139], s[84:85], v[34:35] op_sel:[0,0,0] op_sel_hi:[1,0,1]\n"
      "v_pk_fma_f32 v[40:41], v[136:137], s[48:49], v[40:41] op_sel:[0,1,0] op_sel_hi:[1,1,1]\n"
      "v_pk_fma_f32 v[42:43], v[138:139], s[48:49], v[42:43] op_sel:[0,1,0] op_sel_hi:[1,1,1]\n"
      "s_waitcnt lgkmcnt(3)\n"
      "v_pk_fma_f32 v[16:17], v[140:141], s[64:65], v[16:17] op_sel:[0,0,0] op_sel_hi:[1,0,1]\n"
      "v_pk_fma_f32 v[18:19], v[142:143], s[64:65], v[18:19] op_sel:[0,0,0] op_sel_hi:[1,0,1]\n"
      "v_pk_fma_f32 v[24:25], v[140:141], s[68:69], v[24:25] op_sel:[0,1,0] op_sel_hi:[1,1,1]\n"
      "v_pk_fma_f32 v[26:27], v[142:143], s[68:69], v[26:27] op_sel:[0,1,0] op_sel_hi:[1,1,1]\n"
      "v_pk_fma_f32 v[32:33], v[140:141], s[84:85], v[32:33] op_sel:[0,1,0] op_sel_hi:[1,1,1]\n"
      "v_pk_fma_f32 v[34:35], v[142:143], s[84:85], v[34:35] op_sel:[0,1,0] op_sel_hi:[1,1,1]\n"
      "v_pk_add_f32 v[40:41], v[40:41], v[140:141]\n"
      "v_pk_add_f32 v[42:43], v[42:43], v[142:143]\n"
      "s_waitcnt lgkmcnt(2)\n"
      "v_pk_fma_f32 v[32:33], v[144:145], s[44:45], v[32:33] op_sel:[0,0,0] op_sel_hi:[1,0,1]\n"
      "v_pk_fma_f32 v[34:35], v[146:147], s[44:45], v[34:35] op_sel:[0,0,0] op_sel_hi:[1,0,1]\n"
      "v_pk_fma_f32 v[40:41], v[144:145], s[50:51], v[40:41] op_sel:[0,0,0] op_sel_hi:[1,0,1]\n"
      "v_pk_fma_f32 v[42:43], v[146:147], s[50:51], v[42:43] op_sel:[0,0,0] op_sel_hi:[1,0,1]\n"
      "s_waitcnt lgkmcnt(1)\n"
      "v_pk_fma_f32 v[16:17], v[148:149], s[64:65], v[16:17] op_sel:[0,1,0] op_sel_hi:[1,1,1]\n"
      "v_pk_fma_f32 v[18:19], v[150:151], s[64:65], v[18:19] op_sel:[0,1,0] op_sel_hi:[1,1,1]\n"
      "v_pk_fma_f32 v[32:33], v[148:149], s[44:45], v[32:33] op_sel:[0,1,0] op_sel_hi:[1,1,1]\n"
      "v_pk_fma_f32 v[34:35], v[150:151], s[44:45], v[34:35] op_sel:[0,1,0] op_sel_hi:[1,1,1]\n"
      "s_waitcnt lgkmcnt(0)\n"
      "v_pk_fma_f32 v[32:33], v[152:153], s[46:47], v[32:33] op_sel:[0,0,0] op_sel_hi:[1,0,1]\n"
      "v_pk_fma_f32 v[34:35], v[154:155], s[46:47], v[34:35] op_sel:[0,0,0] op_sel_hi:[1,0,1]\n"
      "s_nop 1\n"
      "v_mov_b32_dpp v164, v16 row_newbcast:0 row_mask:0xf bank_mask:0xf\n"
      "v_mov_b32_dpp v165, v17 row_newbcast:0 row_mask:0xf bank_mask:0xf\n"
      "v_mov_b32_dpp v166, v18 row_newbcast:0 row_mask:0xf bank_mask:0xf\n"
      "v_mov_b32_dpp v167, v19 row_newbcast:0 row_mask:0xf bank_mask:0xf\n"
      "v_pk_add_f32 v[168:169], v[16:17], v[164:165] neg_lo:[0,1] neg_hi:[0,1]\n"
      "v_pk_add_f32 v[170:171], v[18:19], v[166:167] neg_lo:[0,1] neg_hi:[0,1]\n"
      "v_pk_add_f32 v[172:173], v[24:25], v[164:165] neg_lo:[0,1] neg_hi:[0,1]\n"
      "v_pk_add_f32 v[174:175], v[26:27], v[166:167] neg_lo:[0,1] neg_hi:[0,1]\n"
      "v_pk_add_f32 v[176:177], v[32:33], v[164:165] neg_lo:[0,1] neg_hi:[0,1]\n"
      "v_pk_add_f32 v[178:179], v[34:35], v[166:167] neg_lo:[0,1] neg_hi:[0,1]\n"
      "v_pk_add_f32 v[180:181], v[40:41], v[164:165] neg_lo:[0,1] neg_hi:[0,1]\n"
      "v_pk_add_f32 v[182:183], v[42:43], v[166:167] neg_lo:[0,1] neg_hi:[0,1]\n"
      "s_cmp_eq_u64 s[100:101], -1\n"
      "s_cbranch_scc1 LMK1%=\n"
      "s_not_b64 exec, s[94:95]\n"
      "v_mov_b64 v[168:169], 0\n"
      "v_mov_b64 v[170:171], 0\n"
      "s_not_b64 exec, s[96:97]\n"
      "v_mov_b64 v[172:173], 0\n"
      "v_mov_b64 v[174:175], 0\n"
      "s_not_b64 exec, s[98:99]\n"
      "v_mov_b64 v[176:177], 0\n"
      "v_mov_b64 v[178:179], 0\n"
      "s_not_b64 exec, s[100:101]\n"
      "v_mov_b64 v[180:181], 0\n"
      "v_mov_b64 v[182:183], 0\n"
      "s_mov_b64 exec, -1\n"
      "LMK1%=:\n"
      "v_pk_add_f32 v[188:189], v[168:169], v[172:173]\n"
      "v_pk_add_f32 v[190:191], v[170:171], v[174:175]\n"
      "v_pk_mul_f32 v[196:197], v[168:169], v[168:169]\n"
      "v_pk_mul_f32 v[198:199], v[170:171], v[170:171]\n"
      "v_pk_add_f32 v[156:157], v[176:177], v[180:181]\n"
      "v_pk_add_f32 v[158:159], v[178:179], v[182:183]\n"
      "v_pk_fma_f32 v[196:197], v[172:173], v[172:173], v[196:197]\n"
      "v_pk_fma_f32 v[198:199], v[174:175], v[174:175], v[198:199]\n"
      "v_pk_add_f32 v[188:189], v[188:189], v[156:157]\n"
      "v_pk_add_f32 v[190:191], v[190:191], v[158:159]\n"
      "v_pk_fma_f32 v[196:197], v[176:177], v[176:177], v[196:197]\n"
      "v_pk_fma_f32 v[198:199], v[178:179], v[178:179], v[198:199]\n"
      "v_pk_fma_f32 v[196:197], v[180:181], v[180:181], v[196:197]\n"
      "v_pk_fma_f32 v[198:199], v[182:183], v[182:183], v[198:199]\n"
      "s_nop 1\n"
      "v_add_f32_dpp v184, v184, v184 row_ror:8 row_mask:0xf bank_mask:0x3\n"
      "v_add_f32_dpp v185, v185, v185 row_ror:8 row_mask:0xf bank_mask:0x3\n"
      "v_add_f32_dpp v186, v186, v186 row_ror:8 row_mask:0xf bank_mask:0x3\n"
      "v_add_f32_dpp v187, v187, v187 row_ror:8 row_mask:0xf bank_mask:0x3\n"
      "v_add_f32_dpp v188, v188, v188 row_ror:8 row_mask:0xf bank_mask:0x3\n"
      "v_add_f32_dpp v189, v189, v189 row_ror:8 row_mask:0xf bank_mask:0x3\n"
      "v_add_f32_dpp v190, v190, v190 row_ror:8 row_mask:0xf bank_mask:0x3\n"
      "v_add_f32_dpp v191, v191, v191 row_ror:8 row_mask:0xf bank_mask:0x3\n"
      "v_add_f32_dpp v184, v192, v192 row_ror:8 row_mask:0xf bank_mask:0xc\n"
      "v_add_f32_dpp v185, v193, v193 row_ror:8 row_mask:0xf bank_mask:0xc\n"
      "v_add_f32_dpp v186, v194, v194 row_ror:8 row_mask:0xf bank_mask:0xc\n"
      "v_add_f32_dpp v187, v195, v195 row_ror:8 row_mask:0xf bank_mask:0xc\n"
      "v_add_f32_dpp v188, v196, v196 row_ror:8 row_mask:0xf bank_mask:0xc\n"
      "v_add_f32_dpp v189, v197, v197 row_ror:8 row_mask:0xf bank_mask:0xc\n"
      "v_add_f32_dpp v190, v198, v198 row_ror:8 row_mask:0xf bank_mask:0xc\n"
      "v_add_f32_dpp v191, v199, v199 row_ror:8 row_mask:0xf bank_mask:0xc\n"
      "v_add_f32_dpp v184, v184, v184 row_half_mirror row_mask:0xf bank_mask:0x5\n"
      "v_add_f32_dpp v185, v185, v185 row_half_mirror row_mask:0xf bank_mask:0x5\n"
      "v_add_f32_dpp v186, v186, v186 row_half_mirror row_mask:0xf bank_mask:0x5\n"
      "v_add_f32_dpp v187, v187, v187 row_half_mirror row_mask:0xf bank_mask:0x5\n"
      "v_add_f32_dpp v184, v188, v188 row_half_mirror row_mask:0xf bank_mask:0xa\n"
      "v_add_f32_dpp v185, v189, v189 row_half_mirror row_mask:0xf bank_mask:0xa\n"
      "v_add_f32_dpp v186, v190, v190 row_half_mirror row_mask:0xf bank_mask:0xa\n"
      "v_add_f32_dpp v187, v191, v191 row_half_mirror row_mask:0xf bank_mask:0xa\n"
      "v_add_f32_dpp v184, v184, v184 quad_perm:[1,0,3,2] row_mask:0xf bank_mask:0xf\n"
      "v_add_f32_dpp v185, v185, v185 quad_perm:[1,0,3,2] row_mask:0xf bank_mask:0xf\n"
      "v_add_f32_dpp v186, v186, v186 quad_perm:[1,0,3,2] row_mask:0xf bank_mask:0xf\n"
      "v_add_f32_dpp v187, v187, v187 quad_perm:[1,0,3,2] row_mask:0xf bank_mask:0xf\n"
      "s_mov_b32 exec_lo, 0x00010001\n"
      "s_mov_b32 exec_hi, 0x00010001\n"
      "ds_write_b128 v236, v[160:163] offset:2048\n"
      "ds_write_b128 v236, v[164:167] offset:2064\n"
      "s_mov_b64 exec, -1\n"
      "s_nop 4\n"
      "v_add_f32_dpp v184, v184, v184 quad_perm:[2,3,0,1] row_mask:0xf bank_mask:0xf\n"
      "v_add_f32_dpp v185, v185, v185 quad_perm:[2,3,0,1] row_mask:0xf bank_mask:0xf\n"
      "v_add_f32_dpp v186, v186, v186 quad_perm:[2,3,0,1] row_mask:0xf bank_mask:0xf\n"
      "v_add_f32_dpp v187, v187, v187 quad_perm:[2,3,0,1] row_mask:0xf bank_mask:0xf\n"
      "s_mov_b32 exec_lo, 0x11111111\n"
      "s_mov_b32 exec_hi, 0x11111111\n"
      "ds_write_b128 v235, v[184:187]\n"
      "s_mov_b64 exec, -1\n"
      "s_waitcnt lgkmcnt(0)\n"
      "s_barrier\n"
      "s_cmp_lg_u32 %[wv], 4\n"
      "s_cbranch_scc1 LFIN%=\n"
      "ds_read_b32 v84, v213\n"
      "ds_read2st64_b32 v[80:81], v203 offset0:0 offset1:1\n"
      "ds_read2st64_b32 v[82:83], v203 offset0:2 offset1:3\n"
      "ds_read2st64_b32 v[72:73], v202 offset0:0 offset1:1\n"
      "ds_read2st64_b32 v[74:75], v202 offset0:2 offset1:3\n"
      "v_add_u32 v85, 32, v202\n"
      "ds_read2st64_b32 v[76:77], v85 offset0:0 offset1:1\n"
      "ds_read2st64_b32 v[78:79], v85 offset0:2 offset1:3\n"
      "s_waitcnt lgkmcnt(4)\n"
      "v_sub_f32 v80, v80, v84\n"
      "v_sub_f32 v81, v81, v84\n"
      "v_sub_f32 v82, v82, v84\n"
      "v_sub_f32 v83, v83, v84\n"
      "v_mul_f32 v88, v204, v80\n"
      "v_mul_f32 v89, v205, v81\n"
      "v_mul_f32 v90, v206, v82\n"
      "v_mul_f32 v91, v207, v83\n"
      "s_waitcnt lgkmcnt(2)\n"
      "v_add_f32 v88, v88, v72\n"
      "v_add_f32 v89, v89, v73\n"
      "v_add_f32 v90, v90, v74\n"
      "v_add_f32 v91, v91, v75\n"
      "v_add_f32 v72, v72, v88\n"
      "v_add_f32 v73, v73, v89\n"
      "v_add_f32 v74, v74, v90\n"
      "v_add_f32 v75, v75, v91\n"
      "s_waitcnt lgkmcnt(0)\n"
      "v_fmac_f32 v76, v80, v72\n"
      "v_fmac_f32 v77, v81, v73\n"
      "v_fmac_f32 v78, v82, v74\n"
      "v_fmac_f32 v79, v83, v75\n"
      "v_add_f32 v88, v88, v89\n"
      "v_add_f32 v90, v90, v91\n"
      "v_add_f32 v76, v76, v77\n"
      "v_add_f32 v78, v78, v79\n"
      "v_add_f32 v88, v88, v90\n"
      "v_add_f32 v76, v76, v78\n"
      "v_mov_b32 v89, v88\n"
      "v_mov_b32 v77, v76\n"
      "s_nop 1\n"
      "v_permlane32_swap_b32 v88, v89\n"
      "v_permlane32_swap_b32 v76, v77\n"
      "s_nop 0\n"
      "v_add_f32 v88, v88, v89\n"
      "v_add_f32 v76, v76, v77\n"
      "v_mul_f32 v89, s92, v88\n"
      "v_add_f32 v92, v84, v89\n"
      "v_fma_f32 v94, -v88, v89, v76\n"
      "v_mov_b32 v93, v208\n"
      "v_mov_b32 v95, v209\n"
      "s_mov_b32 exec_hi, 0\n"
      "buffer_store_dwordx4 v[92:95], v210, s[88:91], 0 offen sc1\n"
      "s_mov_b32 exec_hi, -1\n"
      "s_mov_b32 s93, 0x200000\n"
      "s_movk_i32 s87, 0x1000\n"
      "buffer_load_dwordx4 v[104:107], v211, s[88:91], 0 offen offset:0 sc1\n"
      "buffer_load_dwordx4 v[108:111], v211, s[88:91], 0 offen offset:1024 sc1\n"
      "buffer_load_dwordx4 v[112:115], v211, s[88:91], 0 offen offset:2048 sc1\n"
      "buffer_load_dwordx4 v[116:119], v211, s[88:91], 0 offen offset:3072 sc1\n"
      "buffer_load_dwordx4 v[120:123], v211, s[88:91], s87 offen offset:0 sc1\n"
      "buffer_load_dwordx4 v[124:127], v211, s[88:91], s87 offen offset:1024 sc1\n"
      "buffer_load_dwordx4 v[128:131], v211, s[88:91], s87 offen offset:2048 sc1\n"
      "buffer_load_dwordx4 v[132:135], v211, s[88:91], s87 offen offset:3072 sc1\n"
      "s_sleep 10\n"
      "LGP%=:\n"
      "buffer_load_dwordx4 v[136:139], v211, s[88:91], 0 offen offset:0 sc1\n"
      "buffer_load_dwordx4 v[140:143], v211, s[88:91], 0 offen offset:1024 sc1\n"
      "buffer_load_dwordx4 v[144:147], v211, s[88:91], 0 offen offset:2048 sc1\n"
      "buffer_load_dwordx4 v[148:151], v211, s[88:91], 0 offen offset:3072 sc1\n"
      "buffer_load_dwordx4 v[152:155], v211, s[88:91], s87 offen offset:0 sc1\n"
      "buffer_load_dwordx4 v[156:159], v211, s[88:91], s87 offen offset:1024 sc1\n"
      "buffer_load_dwordx4 v[160:163], v211, s[88:91], s87 offen offset:2048 sc1\n"
      "buffer_load_dwordx4 v[164:167], v211, s[88:91], s87 offen offset:3072 sc1\n"
      "s_sub_u32 s93, s93, 1\n"
      "s_waitcnt vmcnt(15)\n"
      "v_xor_b32 v105, v105, v208\n"
      "v_xor_b32 v107, v107, v209\n"
      "v_or_b32 v86, v105, v107\n"
      "v_sub_f32 v104, v104, v92\n"
      "v_mul_f32 v105, 0x43fa0000, v104\n"
      "v_fmac_f32 v106, v105, v104\n"
      "v_mov_b32 v97, v105\n"
      "v_mov_b32 v98, v106\n"
      "s_waitcnt vmcnt(14)\n"
      "v_xor_b32 v109, v109, v208\n"
      "v_xor_b32 v111, v111, v209\n"
      "v_or3_b32 v86, v86, v109, v111\n"
      "v_sub_f32 v108, v108, v92\n"
      "v_mul_f32 v109, 0x43fa0000, v108\n"
      "v_fmac_f32 v110, v109, v108\n"
      "v_add_f32 v97, v97, v109\n"
      "v_add_f32 v98, v98, v110\n"
      "s_waitcnt vmcnt(13)\n"
      "v_xor_b32 v113, v113, v208\n"
      "v_xor_b32 v115, v115, v209\n"
      "v_or3_b32 v86, v86, v113, v115\n"
      "v_sub_f32 v112, v112, v92\n"
      "v_mul_f32 v113, 0x43fa0000, v112\n"
      "v_fmac_f32 v114, v113, v112\n"
      "v_add_f32 v97, v97, v113\n"
      "v_add_f32 v98, v98, v114\n"
      "s_waitcnt vmcnt(12)\n"
      "v_xor_b32 v117, v117, v208\n"
      "v_xor_b32 v119, v119, v209\n"
      "v_or3_b32 v86, v86, v117, v119\n"
      "v_sub_f32 v116, v116, v92\n"
      "v_mul_f32 v117, 0x43fa0000, v116\n"
      "v_fmac_f32 v118, v117, v116\n"
      "v_add_f32 v97, v97, v117\n"
      "v_add_f32 v98, v98, v118\n"
      "s_waitcnt vmcnt(11)\n"
      "v_xor_b32 v121, v121, v208\n"
      "v_xor_b32 v123, v123, v209\n"
      "v_or3_b32 v86, v86, v121, v123\n"
      "v_sub_f32 v120, v120, v92\n"
      "v_mul_f32 v121, 0x43fa0000, v120\n"
      "v_fmac_f32 v122, v121, v120\n"
      "v_add_f32 v97, v97, v121\n"
      "v_add_f32 v98, v98, v122\n"
      "s_waitcnt vmcnt(10)\n"
      "v_xor_b32 v125, v125, v208\n"
      "v_xor_b32 v127, v127, v209\n"
      "v_or3_b32 v86, v86, v125, v127\n"
      "v_sub_f32 v124, v124, v92\n"
      "v_mul_f32 v125, 0x43fa0000, v124\n"
      "v_fmac_f32 v126, v125, v124\n"
      "v_add_f32 v97, v97, v125\n"
      "v_add_f32 v98, v98, v126\n"
      "s_waitcnt vmcnt(9)\n"
      "v_xor_b32 v129, v129, v208\n"
      "v_xor_b32 v131, v131, v209\n"
      "v_or3_b32 v86, v86, v129, v131\n"
      "v_sub_f32 v128, v128, v92\n"
      "v_mul_f32 v129, 0x43fa0000, v128\n"
      "v_fmac_f32 v130, v129, v128\n"
      "v_add_f32 v97, v97, v129\n"
      "v_add_f32 v98, v98, v130\n"
      "s_waitcnt vmcnt(8)\n"
      "v_xor_b32 v133, v133, v208\n"
      "v_xor_b32 v135, v135, v209\n"
      "v_or3_b32 v86, v86, v133, v135\n"
      "v_sub_f32 v132, v132, v92\n"
      "v_mul_f32 v133, v212, v132\n"
      "v_fmac_f32 v134, v133, v132\n"
      "v_add_f32 v97, v97, v133\n"
      "v_add_f32 v98, v98, v134\n"
      "v_cmp_ne_u32 vcc, 0, v86\n"
      "s_cmp_eq_u32 s93, 0\n"
      "s_cbranch_scc1 LGD%=\n"
      "s_nop 1\n"
      "s_cbranch_vccz LGD%=\n"
      "buffer_load_dwordx4 v[104:107], v211, s[88:91], 0 offen offset:0 sc1\n"
      "buffer_load_dwordx4 v[108:111], v211, s[88:91], 0 offen offset:1024 sc1\n"
      "buffer_load_dwordx4 v[112:115], v211, s[88:91], 0 offen offset:2048 sc1\n"
      "buffer_load_dwordx4 v[116:119], v211, s[88:91], 0 offen offset:3072 sc1\n"
      "buffer_load_dwordx4 v[120:123], v211, s[88:91], s87 offen offset:0 sc1\n"
      "buffer_load_dwordx4 v[124:127], v211, s[88:91], s87 offen offset:1024 sc1\n"
      "buffer_load_dwordx4 v[128:131], v211, s[88:91], s87 offen offset:2048 sc1\n"
      "buffer_load_dwordx4 v[132:135], v211, s[88:91], s87 offen offset:3072 sc1\n"
      "s_sub_u32 s93, s93, 1\n"
      "s_waitcnt vmcnt(15)\n"
      "v_xor_b32 v137, v137, v208\n"
      "v_xor_b32 v139, v139, v209\n"
      "v_or_b32 v86, v137, v139\n"
      "v_sub_f32 v136, v136, v92\n"
      "v_mul_f32 v137, 0x43fa0000, v136\n"
      "v_fmac_f32 v138, v137, v136\n"
      "v_mov_b32 v97, v137\n"
      "v_mov_b32 v98, v138\n"
      "s_waitcnt vmcnt(14)\n"
      "v_xor_b32 v141, v141, v208\n"
      "v_xor_b32 v143, v143, v209\n"
      "v_or3_b32 v86, v86, v141, v143\n"
      "v_sub_f32 v140, v140, v92\n"
      "v_mul_f32 v141, 0x43fa0000, v140\n"
      "v_fmac_f32 v142, v141, v140\n"
      "v_add_f32 v97, v97, v141\n"
      "v_add_f32 v98, v98, v142\n"
      "s_waitcnt vmcnt(13)\n"
      "v_xor_b32 v145, v145, v208\n"
      "v_xor_b32 v147, v147, v209\n"
      "v_or3_b32 v86, v86, v145, v147\n"
      "v_sub_f32 v144, v144, v92\n"
      "v_mul_f32 v145, 0x43fa0000, v144\n"
      "v_fmac_f32 v146, v145, v144\n"
      "v_add_f32 v97, v97, v145\n"
      "v_add_f32 v98, v98, v146\n"
      "s_waitcnt vmcnt(12)\n"
      "v_xor_b32 v149, v149, v208\n"
      "v_xor_b32 v151, v151, v209\n"
      "v_or3_b32 v86, v86, v149, v151\n"
      "v_sub_f32 v148, v148, v92\n"
      "v_mul_f32 v149, 0x43fa0000, v148\n"
      "v_fmac_f32 v150, v149, v148\n"
      "v_add_f32 v97, v97, v149\n"
      "v_add_f32 v98, v98, v150\n"
      "s_waitcnt vmcnt(11)\n"
      "v_xor_b32 v153, v153, v208\n"
      "v_xor_b32 v155, v155, v209\n"
      "v_or3_b32 v86, v86, v153, v155\n"
      "v_sub_f32 v152, v152, v92\n"
      "v_mul_f32 v153, 0x43fa0000, v152\n"
      "v_fmac_f32 v154, v153, v152\n"
      "v_add_f32 v97, v97, v153\n"
      "v_add_f32 v98, v98, v154\n"
      "s_waitcnt vmcnt(10)\n"
      "v_xor_b32 v157, v157, v208\n"
      "v_xor_b32 v159, v159, v209\n"
      "v_or3_b32 v86, v86, v157, v159\n"
      "v_sub_f32 v156, v156, v92\n"
      "v_mul_f32 v157, 0x43fa0000, v156\n"
      "v_fmac_f32 v158, v157, v156\n"
      "v_add_f32 v97, v97, v157\n"
      "v_add_f32 v98, v98, v158\n"
      "s_waitcnt vmcnt(9)\n"
      "v_xor_b32 v161, v161, v208\n"
      "v_xor_b32 v163, v163, v209\n"
      "v_or3_b32 v86, v86, v161, v163\n"
      "v_sub_f32 v160, v160, v92\n"
      "v_mul_f32 v161, 0x43fa0000, v160\n"
      "v_fmac_f32 v162, v161, v160\n"
      "v_add_f32 v97, v97, v161\n"
      "v_add_f32 v98, v98, v162\n"
      "s_waitcnt vmcnt(8)\n"
      "v_xor_b32 v165, v165, v208\n"
      "v_xor_b32 v167, v167, v209\n"
      "v_or3_b32 v86, v86, v165, v167\n"
      "v_sub_f32 v164, v164, v92\n"
      "v_mul_f32 v165, v212, v164\n"
      "v_fmac_f32 v166, v165, v164\n"
      "v_add_f32 v97, v97, v165\n"
      "v_add_f32 v98, v98, v166\n"
      "v_cmp_ne_u32 vcc, 0, v86\n"
      "s_cmp_eq_u32 s93, 0\n"
      "s_cbranch_scc1 LGD%=\n"
      "s_nop 1\n"
      "s_cbranch_vccz LGD%=\n"
      "s_branch LGP%=\n"
      "LGD%=:\n"
      "v_mov_b32 v100, v97\n"
      "v_mov_b32 v101, v98\n"
      "v_lshlrev_b32 v200, 16, v200\n"
      "v_lshlrev_b32 v201, 16, v201\n"
      "v_permlane32_swap_b32 v97, v100\n"
      "v_permlane32_swap_b32 v98, v101\n"
      "s_nop 0\n"
      "v_add_f32 v97, v97, v100\n"
      "v_add_f32 v98, v98, v101\n"
      "v_mul_f32 v97, 0x39035177, v97\n"
      "v_mul_f32 v98, 0x39035177, v98\n"
      "v_fma_f32 v98, -v97, v97, v98\n"
      "v_add_f32 v92, v92, v97\n"
      "v_cmp_gt_f32 vcc, 0, v98\n"
      "s_nop 1\n"
      "v_cndmask_b32 v98, v98, 0, vcc\n"
      "v_add_f32 v98, 0x3727c5ac, v98\n"
      "v_rsq_f32 v98, v98\n"
      "s_nop 0\n"
      "v_mul_f32 v98, v98, v200\n"
      "v_fma_f32 v99, -v92, v98, v201\n"
      "s_mov_b32 exec_lo, 0\n"
      "v_mov_b32 v98, v99\n"
      "s_mov_b32 exec_lo, -1\n"
      "ds_write_b32 v214, v98\n"
      "s_cmp_lg_u32 %[ci], 0\n"
      "s_cbranch_scc1 LFIN%=\n"
      "s_mov_b64 exec, 1\n"
      "v_mov_b32 v100, 0\n"
      "global_store_dword v100, v208, %[ep]\n"
      "s_mov_b64 exec, -1\n"
      "LFIN%=:\n"
      "s_mov_b32 s56, 0xbdd2d3e8\n"
      "s_mov_b32 s57, 0xbdd2d3e8\n"
      "v_mov_b32 v176, 0xc0135761\n"
      "v_mov_b32 v177, 0xc0135761\n"
      "s_waitcnt lgkmcnt(0)\n"
      "s_barrier\n"
      "ds_read_b128 v[44:47], v237\n"
      "ds_read_b128 v[52:55], v237 offset:128\n"
      "ds_read_b128 v[48:51], v237 offset:16\n"
      "ds_read_b128 v[56:59], v237 offset:144\n"
      "s_waitcnt lgkmcnt(2)\n"
      "v_pk_fma_f32 v[12:13], v[12:13], v[44:45], v[52:53]\n"
      "v_pk_fma_f32 v[14:15], v[14:15], v[46:47], v[54:55]\n"
      "s_waitcnt lgkmcnt(0)\n"
      "v_pk_fma_f32 v[16:17], v[16:17], v[48:49], v[56:57]\n"
      "v_pk_fma_f32 v[18:19], v[18:19], v[50:51], v[58:59]\n"
      "v_pk_mul_f32 v[60:61], v[12:13], v[12:13]\n"
      "v_pk_mul_f32 v[62:63], v[14:15], v[14:15]\n"
      "v_pk_mul_f32 v[64:65], v[16:17], v[16:17]\n"
      "v_pk_mul_f32 v[66:67], v[18:19], v[18:19]\n"
      "v_pk_fma_f32 v[60:61], v[60:61], s[56:57], v[176:177]\n"
      "v_pk_fma_f32 v[62:63], v[62:63], s[56:57], v[176:177]\n"
      "v_pk_fma_f32 v[64:65], v[64:65], s[56:57], v[176:177]\n"
      "v_pk_fma_f32 v[66:67], v[66:67], s[56:57], v[176:177]\n"
      "v_pk_mul_f32 v[60:61], v[60:61], v[12:13]\n"
      "v_pk_mul_f32 v[62:63], v[62:63], v[14:15]\n"
      "v_pk_mul_f32 v[64:65], v[64:65], v[16:17]\n"
      "v_pk_mul_f32 v[66:67], v[66:67], v[18:19]\n"
      "v_exp_f32 v60, v60\n"
      "v_exp_f32 v61, v61\n"
      "v_exp_f32 v62, v62\n"
      "v_exp_f32 v63, v63\n"
      "v_exp_f32 v64, v64\n"
      "v_exp_f32 v65, v65\n"
      "v_exp_f32 v66, v66\n"
      "v_exp_f32 v67, v67\n"
      "v_pk_add_f32 v[60:61], v[60:61], 1.0 op_sel_hi:[1,0]\n"
      "v_pk_add_f32 v[62:63], v[62:63], 1.0 op_sel_hi:[1,0]\n"
      "v_pk_add_f32 v[64:65], v[64:65], 1.0 op_sel_hi:[1,0]\n"
      "v_pk_add_f32 v[66:67], v[66:67], 1.0 op_sel_hi:[1,0]\n"
      "v_rcp_f32 v60, v60\n"
      "v_rcp_f32 v61, v61\n"
      "v_rcp_f32 v62, v62\n"
      "v_rcp_f32 v63, v63\n"
      "v_rcp_f32 v64, v64\n"
      "v_rcp_f32 v65, v65\n"
      "v_rcp_f32 v66, v66\n"
      "v_rcp_f32 v67, v67\n"
      "v_pk_mul_f32 v[60:61], v[60:61], v[12:13]\n"
      "v_pk_mul_f32 v[62:63], v[62:63], v[14:15]\n"
      "v_pk_mul_f32 v[64:65], v[64:65], v[16:17]\n"
      "v_pk_mul_f32 v[66:67], v[66:67], v[18:19]\n"
      "v_cvt_pk_bf16_f32 v76, v60, v64\n"
      "v_cvt_pk_bf16_f32 v77, v61, v65\n"
      "v_cvt_pk_bf16_f32 v78, v62, v66\n"
      "v_cvt_pk_bf16_f32 v79, v63, v67\n"
      "s_mov_b64 exec, s[94:95]\n"
      "buffer_store_dwordx4 v[76:79], v234, s[40:43], 0 offen offset:0 sc1 nt\n"
      "s_mov_b64 exec, -1\n"
      "v_pk_fma_f32 v[20:21], v[20:21], v[44:45], v[52:53]\n"
      "v_pk_fma_f32 v[22:23], v[22:23], v[46:47], v[54:55]\n"
      "v_pk_fma_f32 v[24:25], v[24:25], v[48:49], v[56:57]\n"
      "v_pk_fma_f32 v[26:27], v[26:27], v[50:51], v[58:59]\n"
      "v_pk_mul_f32 v[68:69], v[20:21], v[20:21]\n"
      "v_pk_mul_f32 v[70:71], v[22:23], v[22:23]\n"
      "v_pk_mul_f32 v[72:73], v[24:25], v[24:25]\n"
      "v_pk_mul_f32 v[74:75], v[26:27], v[26:27]\n"
      "v_pk_fma_f32 v[68:69], v[68:69], s[56:57], v[176:177]\n"
      "v_pk_fma_f32 v[70:71], v[70:71], s[56:57], v[176:177]\n"
      "v_pk_fma_f32 v[72:73], v[72:73], s[56:57], v[176:177]\n"
      "v_pk_fma_f32 v[74:75], v[74:75], s[56:57], v[176:177]\n"
      "v_pk_mul_f32 v[68:69], v[68:69], v[20:21]\n"
      "v_pk_mul_f32 v[70:71], v[70:71], v[22:23]\n"
      "v_pk_mul_f32 v[72:73], v[72:73], v[24:25]\n"
      "v_pk_mul_f32 v[74:75], v[74:75], v[26:27]\n"
      "v_exp_f32 v68, v68\n"
      "v_exp_f32 v69, v69\n"
      "v_exp_f32 v70, v70\n"
      "v_exp_f32 v71, v71\n"
      "v_exp_f32 v72, v72\n"
      "v_exp_f32 v73, v73\n"
      "v_exp_f32 v74, v74\n"
      "v_exp_f32 v75, v75\n"
      "v_pk_add_f32 v[68:69], v[68:69], 1.0 op_sel_hi:[1,0]\n"
      "v_pk_add_f32 v[70:71], v[70:71], 1.0 op_sel_hi:[1,0]\n"
      "v_pk_add_f32 v[72:73], v[72:73], 1.0 op_sel_hi:[1,0]\n"
      "v_pk_add_f32 v[74:75], v[74:75], 1.0 op_sel_hi:[1,0]\n"
      "v_rcp_f32 v68, v68\n"
      "v_rcp_f32 v69, v69\n"
      "v_rcp_f32 v70, v70\n"
      "v_rcp_f32 v71, v71\n"
      "v_rcp_f32 v72, v72\n"
      "v_rcp_f32 v73, v73\n"
      "v_rcp_f32 v74, v74\n"
      "v_rcp_f32 v75, v75\n"
      "v_pk_mul_f32 v[68:69], v[68:69], v[20:21]\n"
      "v_pk_mul_f32 v[70:71], v[70:71], v[22:23]\n"
      "v_pk_mul_f32 v[72:73], v[72:73], v[24:25]\n"
      "v_pk_mul_f32 v[74:75], v[74:75], v[26:27]\n"
      "v_cvt_pk_bf16_f32 v80, v68, v72\n"
      "v_cvt_pk_bf16_f32 v81, v69, v73\n"
      "v_cvt_pk_bf16_f32 v82, v70, v74\n"
      "v_cvt_pk_bf16_f32 v83, v71, v75\n"
      "s_mov_b64 exec, s[96:97]\n"
      "buffer_store_dwordx4 v[80:83], v234, s[40:43], 0 offen offset:512 sc1 nt\n"
      "s_mov_b64 exec, -1\n"
      "v_pk_fma_f32 v[28:29], v[28:29], v[44:45], v[52:53]\n"
      "v_pk_fma_f32 v[30:31], v[30:31], v[46:47], v[54:55]\n"
      "v_pk_fma_f32 v[32:33], v[32:33], v[48:49], v[56:57]\n"
      "v_pk_fma_f32 v[34:35], v[34:35], v[50:51], v[58:59]\n"
      "v_pk_mul_f32 v[60:61], v[28:29], v[28:29]\n"
      "v_pk_mul_f32 v[62:63], v[30:31], v[30:31]\n"
      "v_pk_mul_f32 v[64:65], v[32:33], v[32:33]\n"
      "v_pk_mul_f32 v[66:67], v[34:35], v[34:35]\n"
      "v_pk_fma_f32 v[60:61], v[60:61], s[56:57], v[176:177]\n"
      "v_pk_fma_f32 v[62:63], v[62:63], s[56:57], v[176:177]\n"
      "v_pk_fma_f32 v[64:65], v[64:65], s[56:57], v[176:177]\n"
      "v_pk_fma_f32 v[66:67], v[66:67], s[56:57], v[176:177]\n"
      "v_pk_mul_f32 v[60:61], v[60:61], v[28:29]\n"
      "v_pk_mul_f32 v[62:63], v[62:63], v[30:31]\n"
      "v_pk_mul_f32 v[64:65], v[64:65], v[32:33]\n"
      "v_pk_mul_f32 v[66:67], v[66:67], v[34:35]\n"
      "v_exp_f32 v60, v60\n"
      "v_exp_f32 v61, v61\n"
      "v_exp_f32 v62, v62\n"
      "v_exp_f32 v63, v63\n"
      "v_exp_f32 v64, v64\n"
      "v_exp_f32 v65, v65\n"
      "v_exp_f32 v66, v66\n"
      "v_exp_f32 v67, v67\n"
      "v_pk_add_f32 v[60:61], v[60:61], 1.0 op_sel_hi:[1,0]\n"
      "v_pk_add_f32 v[62:63], v[62:63], 1.0 op_sel_hi:[1,0]\n"
      "v_pk_add_f32 v[64:65], v[64:65], 1.0 op_sel_hi:[1,0]\n"
      "v_pk_add_f32 v[66:67], v[66:67], 1.0 op_sel_hi:[1,0]\n"
      "v_rcp_f32 v60, v60\n"
      "v_rcp_f32 v61, v61\n"
      "v_rcp_f32 v62, v62\n"
      "v_rcp_f32 v63, v63\n"
      "v_rcp_f32 v64, v64\n"
      "v_rcp_f32 v65, v65\n"
      "v_rcp_f32 v66, v66\n"
      "v_rcp_f32 v67, v67\n"
      "v_pk_mul_f32 v[60:61], v[60:61], v[28:29]\n"
      "v_pk_mul_f32 v[62:63], v[62:63], v[30:31]\n"
      "v_pk_mul_f32 v[64:65], v[64:65], v[32:33]\n"
      "v_pk_mul_f32 v[66:67], v[66:67], v[34:35]\n"
      "v_cvt_pk_bf16_f32 v84, v60, v64\n"
      "v_cvt_pk_bf16_f32 v85, v61, v65\n"
      "v_cvt_pk_bf16_f32 v86, v62, v66\n"
      "v_cvt_pk_bf16_f32 v87, v63, v67\n"
      "s_mov_b64 exec, s[98:99]\n"
      "buffer_store_dwordx4 v[84:87], v234, s[40:43], 0 offen offset:1024 sc1 nt\n"
      "s_mov_b64 exec, -1\n"
      "v_pk_fma_f32 v[36:37], v[36:37], v[44:45], v[52:53]\n"
      "v_pk_fma_f32 v[38:39], v[38:39], v[46:47], v[54:55]\n"
      "v_pk_fma_f32 v[40:41], v[40:41], v[48:49], v[56:57]\n"
      "v_pk_fma_f32 v[42:43], v[42:43], v[50:51], v[58:59]\n"
      "v_pk_mul_f32 v[68:69], v[36:37], v[36:37]\n"
      "v_pk_mul_f32 v[70:71], v[38:39], v[38:39]\n"
      "v_pk_mul_f32 v[72:73], v[40:41], v[40:41]\n"
      "v_pk_mul_f32 v[74:75], v[42:43], v[42:43]\n"
      "v_pk_fma_f32 v[68:69], v[68:69], s[56:57], v[176:177]\n"
      "v_pk_fma_f32 v[70:71], v[70:71], s[56:57], v[176:177]\n"
      "v_pk_fma_f32 v[72:73], v[72:73], s[56:57], v[176:177]\n"
      "v_pk_fma_f32 v[74:75], v[74:75], s[56:57], v[176:177]\n"
      "v_pk_mul_f32 v[68:69], v[68:69], v[36:37]\n"
      "v_pk_mul_f32 v[70:71], v[70:71], v[38:39]\n"
      "v_pk_mul_f32 v[72:73], v[72:73], v[40:41]\n"
      "v_pk_mul_f32 v[74:75], v[74:75], v[42:43]\n"
      "v_exp_f32 v68, v68\n"
      "v_exp_f32 v69, v69\n"
      "v_exp_f32 v70, v70\n"
      "v_exp_f32 v71, v71\n"
      "v_exp_f32 v72, v72\n"
      "v_exp_f32 v73, v73\n"
      "v_exp_f32 v74, v74\n"
      "v_exp_f32 v75, v75\n"
      "v_pk_add_f32 v[68:69], v[68:69], 1.0 op_sel_hi:[1,0]\n"
      "v_pk_add_f32 v[70:71], v[70:71], 1.0 op_sel_hi:[1,0]\n"
      "v_pk_add_f32 v[72:73], v[72:73], 1.0 op_sel_hi:[1,0]\n"
      "v_pk_add_f32 v[74:75], v[74:75], 1.0 op_sel_hi:[1,0]\n"
      "v_rcp_f32 v68, v68\n"
      "v_rcp_f32 v69, v69\n"
      "v_rcp_f32 v70, v70\n"
      "v_rcp_f32 v71, v71\n"
      "v_rcp_f32 v72, v72\n"
      "v_rcp_f32 v73, v73\n"
      "v_rcp_f32 v74, v74\n"
      "v_rcp_f32 v75, v75\n"
      "v_pk_mul_f32 v[68:69], v[68:69], v[36:37]\n"
      "v_pk_mul_f32 v[70:71], v[70:71], v[38:39]\n"
      "v_pk_mul_f32 v[72:73], v[72:73], v[40:41]\n"
      "v_pk_mul_f32 v[74:75], v[74:75], v[42:43]\n"
      "v_cvt_pk_bf16_f32 v88, v68, v72\n"
      "v_cvt_pk_bf16_f32 v89, v69, v73\n"
      "v_cvt_pk_bf16_f32 v90, v70, v74\n"
      "v_cvt_pk_bf16_f32 v91, v71, v75\n"
      "s_mov_b64 exec, s[100:101]\n"
      "buffer_store_dwordx4 v[88:91], v234, s[40:43], 0 offen offset:1536 sc1 nt\n"
      "s_mov_b64 exec, -1\n"
      "LENDK%=:\n"
      : [tag] "=&s"(tag)
      : [wv] "s"(wv), [rt] "s"(rt), [cb] "s"(cb), [ci] "s"(rtg), [cj] "s"(idx * 8 + wv), [ep] "s"(ws + EP_OFF + cb * 128),
        [xlo] "s"((unsigned)xb), [xhi] "s"((unsigned)(xb >> 32) & 0xffffu), [wlo] "s"((unsigned)wb), [whi] "s"((unsigned)(wb >> 32) & 0xffffu),
        [glo] "s"((unsigned)gb), [ghi] "s"((unsigned)(gb >> 32) & 0xffffu), [flo] "s"((unsigned)fb), [fhi] "s"((unsigned)(fb >> 32) & 0xffffu),
        [rlo] "s"((unsigned)rb), [rhi] "s"((unsigned)(rb >> 32) & 0xffffu), [olo] "s"((unsigned)ob), [ohi] "s"((unsigned)(ob >> 32) & 0xffffu),
        [gam] "s"(gam), [bet] "s"(bet)
      : "vcc","v0","v1","v2","v3","v4","v5","v6","v7","v8","v9","v10","v11","v12","v13","v14","v15","v16","v17","v18","v19","v20","v21","v22","v23","v24","v25","v26","v27","v28","v29","v30","v31","v32","v33","v34","v35","v36","v37","v38","v39","v40","v41","v42","v43","v44","v45","v46","v47","v48","v49","v50","v51","v52","v53","v54","v55","v56","v57","v58","v59","v60","v61","v62","v63","v64","v65","v66","v67","v68","v69","v70","v71","v72","v73","v74","v75","v76","v77","v78","v79","v80","v81","v82","v83","v84","v85","v86","v87","v88","v89","v90","v91","v92","v93","v94","v95","v96","v97","v98","v99","v100","v101","v102","v103","v104","v105","v106","v107","v108","v109","v110","v111","v112","v113","v114","v115","v116","v117","v118","v119","v120","v121","v122","v123","v124","v125","v126","v127","v128","v129","v130","v131","v132","v133","v134","v135","v136","v137","v138","v139","v140","v141","v142","v143","v144","v145","v146","v147","v148","v149","v150","v151","v152","v153","v154","v155","v156","v157","v158","v159","v160","v161","v162","v163","v164","v165","v166","v167","v168","v169","v170","v171","v172","v173","v174","v175","v176","v177","v178","v179","v180","v181","v182","v183","v184","v185","v186","v187","v188","v189","v190","v191","v192","v193","v194","v195","v196","v197","v198","v199","v200","v201","v202","v203","v204","v205","v206","v207","v208","v209","v210","v211","v212","v213","v214","v215","v216","v217","v218","v219","v220","v221","v222","v223","v224","v225","v226","v227","v228","v229","v230","v231","v232","v233","v234","v235","v236","v237","v238","v239","v240","v241","v242","v243","v244","v245","v246","v247","v248","v249","v250","v251","v252","v253","v254","v255","s40","s41","s42","s43","s44","s45","s46","s47","s48","s49","s50","s51","s52","s53","s54","s55","s56","s57","s58","s59","s60","s61","s62","s63","s64","s65","s66","s67","s68","s69","s70","s71","s72","s73","s74","s75","s76","s77","s78","s79","s80","s81","s82","s83","s84","s85","s86","s87","s88","s89","s90","s91","s92","s93","s94","s95","s96","s97","s98","s99","s100","s101","scc","memory");
}

extern "C" void kernel_launch(void* const* d_in, const int* in_sizes, int n_in, void* d_out, int out_size, void* d_ws, size_t ws_size,
                              hipStream_t stream) {
  (void)hipFuncSetAttribute((const void*)conv_gn_gelu_k, hipFuncAttributeMaxDynamicSharedMemorySize, 163840);
  conv_gn_gelu_k<<<256, 512, 163840, stream>>>((const unsigned char*)d_in[0], (const unsigned char*)d_in[1], (unsigned char*)d_ws,
                                               (const unsigned short*)d_in[2], (const unsigned short*)d_in[3], (unsigned char*)d_out);
}
```
